```python
import torch

def _softmax(x, axis=-1):
    return torch.softmax(x, dim=axis)
def _gelu(x):
    return torch.nn.functional.gelu(x, approximate="tanh")
def _silu(x):
    return torch.nn.functional.silu(x)
def _sigmoid(x):
    return torch.sigmoid(x)
def _layernorm(x, axis=-1, eps=1e-5):
    m = x.mean(dim=axis, keepdim=True)
    v = x.var(dim=axis, keepdim=True, unbiased=False)
    return (x - m) / torch.sqrt(v + eps)
def _scatter_add(tgt, idx, src, axis=0):
    return tgt.clone().index_add_(axis, idx, src)
def _segment_sum(src, seg, n):
    out = torch.zeros((n,) + src.shape[1:], dtype=src.dtype)
    return out.index_add_(0, seg, src)
def _pad(x, pw):
    flat = tuple(v for pair in reversed(pw) for v in pair)
    return torch.nn.functional.pad(x, flat)

kernel_name = 'standalone__sparse_matmul_formats_ell__sparse_mat'

def setup_inputs():

    _w = (lambda a: (lambda t: t.to(torch.bfloat16) if t.is_floating_point() else t)(torch.as_tensor(a)))
    M = N = 65536
    D = 64
    K = max(1, int(0.001 * N))
    col_idx = (lambda lo, hi, *s: torch.randint(lo, hi, s))(0, N, M, K)
    vals = (lambda *s: torch.randn(*s, dtype=torch.bfloat16) * 0.05)(M, K)
    lengths = (lambda lo, hi, *s: torch.randint(lo, hi, s))(1, K + 1, M)
    mask = (torch.arange(K)[None, :] < lengths[:, None]).to(vals.dtype)
    vals = vals * mask
    b = (lambda *s: torch.randn(*s, dtype=torch.bfloat16) * 0.05)(N, D)
    return {
        "vals": _w(vals),
        "col_idx": _w(col_idx),
        "b": _w(b),
    }

def reference(vals, col_idx, b):
    gathered = b[col_idx]
    return torch.einsum("mk,mkd->md", vals, gathered)
```

```cpp
#include <hip/hip_runtime.h>
#include <hip/hip_bf16.h>
#include <stdint.h>

typedef __attribute__((ext_vector_type(4))) unsigned u32x4;

__device__ __forceinline__ float bf_lo(unsigned u) { return __uint_as_float(u << 16); }
__device__ __forceinline__ float bf_hi(unsigned u) { return __uint_as_float(u & 0xffff0000u); }

__global__ __launch_bounds__(256) void spmm_naive(const uint16_t* __restrict__ vals, const int* __restrict__ col,
                                                   const uint16_t* __restrict__ B, uint16_t* __restrict__ out,
                                                   int M, int K) {
    const int tid = blockIdx.x * 256 + threadIdx.x;
    const int row = tid >> 3;
    const int sub = tid & 7;
    if (row >= M) return;
    const uint16_t* vr = vals + (size_t)row * K;
    const int* cr = col + (size_t)row * K;
    float acc[8];
#pragma unroll
    for (int i = 0; i < 8; ++i) acc[i] = 0.f;
    for (int j0 = 0; j0 < K; j0 += 8) {
        int j = j0 + sub;
        unsigned v = 0; int c = 0;
        if (j < K) { v = vr[j]; c = cr[j]; }
#pragma unroll
        for (int t = 0; t < 8; ++t) {
            unsigned vt = __shfl(v, t, 8);
            int ct = __shfl(c, t, 8);
            if ((vt & 0x7fff) != 0) {
                u32x4 b = *(const u32x4*)(B + (size_t)ct * 64 + sub * 8);
                float f = __uint_as_float(vt << 16);
                acc[0] += f * bf_lo(b[0]); acc[1] += f * bf_hi(b[0]);
                acc[2] += f * bf_lo(b[1]); acc[3] += f * bf_hi(b[1]);
                acc[4] += f * bf_lo(b[2]); acc[5] += f * bf_hi(b[2]);
                acc[6] += f * bf_lo(b[3]); acc[7] += f * bf_hi(b[3]);
            }
        }
    }
    u32x4 o;
#pragma unroll
    for (int i = 0; i < 4; ++i) {
        __hip_bfloat16 lo = __float2bfloat16(acc[2 * i]);
        __hip_bfloat16 hi = __float2bfloat16(acc[2 * i + 1]);
        o[i] = (unsigned)__bfloat16_as_ushort(lo) | ((unsigned)__bfloat16_as_ushort(hi) << 16);
    }
    *(u32x4*)(out + (size_t)row * 64 + sub * 8) = o;
}

extern "C" void kernel_launch(void* const* d_in, const int* in_sizes, int n_in,
                              void* d_out, int out_size, void* d_ws, size_t ws_size,
                              hipStream_t stream) {
    const uint16_t* vals = (const uint16_t*)d_in[0];
    const int* col = (const int*)d_in[1];
    const uint16_t* B = (const uint16_t*)d_in[2];
    uint16_t* out = (uint16_t*)d_out;
    int M = out_size / 64;
    int K = in_sizes[0] / M;
    spmm_naive<<<(M * 8 + 255) / 256, 256, 0, stream>>>(vals, col, B, out, M, K);
}
```

```cpp
#include <hip/hip_runtime.h>
#include <stdint.h>
#include <stdio.h>

typedef __attribute__((ext_vector_type(4))) unsigned u32x4;
typedef __attribute__((ext_vector_type(2))) unsigned u32x2;
typedef __attribute__((ext_vector_type(4))) short s16x4;
typedef __attribute__((ext_vector_type(4))) float f32x4;
typedef __attribute__((ext_vector_type(2))) float f32x2;
typedef __attribute__((ext_vector_type(2))) __bf16 bf16x2;

constexpr int KS = 65;
constexpr int ROWS = 256;
constexpr int NTHR = 1024;
constexpr int LCAP = 132;

constexpr int OFF_VALS = 0;
constexpr int OFF_IDX = ROWS * KS * 2;
constexpr int OFF_MASK = OFF_IDX + 128 * LCAP * 4;
constexpr int OFF_HIST = OFF_MASK + ROWS * 8;
constexpr int OFF_RMAP = OFF_HIST + 1024;
constexpr int OFF_NB = OFF_RMAP + 512;
constexpr int OFF_DUMP = OFF_NB + 16;
constexpr int LDS_BYTES = OFF_DUMP + 256;

__device__ __forceinline__ void dma16_nt(const void* gbase, unsigned goff, unsigned lds_dst) {
    unsigned keep;
    asm volatile("s_mov_b32 %0, m0\n\ts_mov_b32 m0, %3\n\ts_nop 0\n\tglobal_load_lds_dwordx4 %1, %2 nt\n\ts_mov_b32 m0, %0"
                 : "=&s"(keep) : "v"(goff), "s"(gbase), "s"(lds_dst) : "memory");
}
__device__ __forceinline__ void dma4(const void* gbase, unsigned goff, unsigned lds_dst) {
    unsigned keep;
    asm volatile("s_mov_b32 %0, m0\n\ts_mov_b32 m0, %3\n\ts_nop 0\n\tglobal_load_lds_dword %1, %2\n\ts_mov_b32 m0, %0"
                 : "=&s"(keep) : "v"(goff), "s"(gbase), "s"(lds_dst) : "memory");
}
__device__ __forceinline__ void gload(u32x4& d, const void* base, unsigned off) {
    asm volatile("global_load_dwordx4 %0, %1, %2" : "=v"(d) : "v"(off), "s"(base));
}
template <int N> __device__ __forceinline__ void wait_vm(u32x4& a, u32x4& b, u32x4& c, u32x4& d) {
    asm volatile("s_waitcnt vmcnt(%4)" : "+v"(a), "+v"(b), "+v"(c), "+v"(d) : "i"(N));
}
__device__ __forceinline__ void bar() { asm volatile("s_waitcnt lgkmcnt(0)\n\ts_barrier" ::: "memory"); }

template <int CTRL, int BANK> __device__ __forceinline__ unsigned dpp(unsigned old, unsigned x) {
    return __builtin_amdgcn_update_dpp(old, x, CTRL, 0xf, BANK, true);
}

__global__ __launch_bounds__(NTHR) void spmm_ell_kernel(const uint16_t* __restrict__ vals, const int* __restrict__ col,
                                                        const uint16_t* __restrict__ Bm, uint16_t* __restrict__ out) {
    __shared__ __attribute__((aligned(16))) char smem[LDS_BYTES];
    const int tid = threadIdx.x;
    const int lane = tid & 63;
    const int w = __builtin_amdgcn_readfirstlane(tid >> 6);
    const int rowBase = blockIdx.x * ROWS;
    const unsigned sbase = (unsigned)(size_t)smem;

    {
        const char* gv = (const char*)vals + (size_t)rowBase * (KS * 2);
        const char* gi = (const char*)col + (size_t)rowBase * (KS * 4);
        const unsigned wl = sbase + w * 1024;
#pragma unroll
        for (int k = 0; k < 2; ++k) dma16_nt(gv, (k * 1024 + tid) * 16, wl + OFF_VALS + k * 16384);
        if (w == 0) { if (lane < 32) dma16_nt(gv, (2048 + lane) * 16, sbase + OFF_VALS + 2048 * 16); }
#pragma unroll
        for (int k = 0; k < 4; ++k) dma16_nt(gi, (k * 1024 + tid) * 16, wl + OFF_IDX + k * 16384);
        if (w == 1) dma16_nt(gi, (4096 + lane) * 16, sbase + OFF_IDX + 4096 * 16);
        if (w >= 4 && w < 8) dma4(Bm, (unsigned)((blockIdx.x * 4 + (w - 4)) * 64 + lane) * 128u, sbase + OFF_DUMP);
    }
    if (tid < 64) *(u32x4*)(smem + OFF_HIST + tid * 16) = u32x4{0, 0, 0, 0};
    if (tid == 64) *(unsigned*)(smem + OFF_NB) = 0;
    asm volatile("s_waitcnt vmcnt(4)" ::: "memory");
    bar();

    {
        unsigned mlo = 0, mhi = 0;
        const uint16_t* vr = (const uint16_t*)(smem + OFF_VALS) + (w * 16) * KS + lane;
#pragma unroll
        for (int rr = 0; rr < 16; ++rr) {
            const unsigned v = vr[rr * KS];
            const unsigned long long bal = __ballot(v != 0);
            mlo = (lane == rr) ? (unsigned)bal : mlo;
            mhi = (lane == rr) ? (unsigned)(bal >> 32) : mhi;
        }
        const int r = w * 16 + (lane & 15);
        const unsigned nz64 = ((const uint16_t*)(smem + OFF_VALS))[r * KS + 64] != 0 ? 1u : 0u;
        const bool valid = lane < 16;
        if (valid) *(u32x2*)(smem + OFF_MASK + r * 8) = u32x2{mlo, mhi};
        unsigned key = __builtin_popcount(mlo) + __builtin_popcount(mhi) + nz64;
        key = key > 63 ? 63 : key;
        unsigned eq = 0xffffu;
#pragma unroll
        for (int b = 0; b < 6; ++b) {
            const int bit = -(int)((key >> b) & 1);
            const unsigned bal = (unsigned)__ballot(bit != 0);
            eq &= ~(bal ^ (unsigned)bit);
        }
        const unsigned rank = __builtin_amdgcn_mbcnt_lo(eq, 0);
        if (valid && rank == 0) *(uint8_t*)(smem + OFF_HIST + key * 16 + w) = (uint8_t)__builtin_popcount(eq);
        bar();
        const u32x4 h = *(const u32x4*)(smem + OFF_HIST + lane * 16);
        unsigned tot = __builtin_amdgcn_sad_u8(h[0], 0, 0);
        tot = __builtin_amdgcn_sad_u8(h[1], 0, tot);
        tot = __builtin_amdgcn_sad_u8(h[2], 0, tot);
        tot = __builtin_amdgcn_sad_u8(h[3], 0, tot);
        unsigned inc = tot;
        inc += dpp<0x111, 0xf>(0, inc);
        inc += dpp<0x112, 0xf>(0, inc);
        inc += dpp<0x114, 0xf>(0, inc);
        inc += dpp<0x118, 0xf>(0, inc);
        inc += __builtin_amdgcn_update_dpp(0, inc, 0x142, 0xa, 0xf, true);
        inc += __builtin_amdgcn_update_dpp(0, inc, 0x143, 0xc, 0xf, true);
        const unsigned exc = inc - tot;
        const unsigned base = __builtin_amdgcn_ds_bpermute(key * 4, exc);
        const u32x4 hk = *(const u32x4*)(smem + OFF_HIST + key * 16);
        unsigned early = 0;
#pragma unroll
        for (int q = 0; q < 4; ++q) {
            const int nb = w - q * 4;
            const unsigned m = nb >= 4 ? 0xffffffffu : (nb <= 0 ? 0u : ((1u << (nb * 8)) - 1u));
            early = __builtin_amdgcn_sad_u8(hk[q] & m, 0, early);
        }
        const unsigned pos = base + early + rank;
        const unsigned slot = pos < 128 ? pos * 2 : (255 - pos) * 2 + 1;
        if (valid) *(uint16_t*)(smem + OFF_RMAP + slot * 2) = (uint16_t)(r | (nz64 << 8));
    }
    if (w >= 4 && w < 8) asm volatile("s_waitcnt vmcnt(1)" ::: "memory"); else asm volatile("s_waitcnt vmcnt(0)" ::: "memory");
    bar();

    const int sub = lane & 7;
    const int q = tid >> 3;
    const unsigned rm = *(const unsigned*)(smem + OFF_RMAP + q * 4);
    const unsigned r0 = rm & 0xff, r1 = (rm >> 16) & 0xff;
    unsigned ent[2][9], aux[2][9], nzb[2];
    unsigned mw[2][2];
    bool act[2][9];
    unsigned len;
    unsigned slo, shi;
    {
        unsigned idx[2][9];
#pragma unroll
        for (int s = 0; s < 2; ++s) {
            const unsigned r = s ? r1 : r0;
            const u32x2 m = *(const u32x2*)(smem + OFF_MASK + r * 8);
            const unsigned long long mm = (((unsigned long long)m[1] << 32) | m[0]) >> sub;
            mw[s][0] = (unsigned)mm; mw[s][1] = (unsigned)(mm >> 32);
            nzb[s] = (sub == 0) ? ((rm >> (s * 16 + 8)) & 1) : 0;
            const int* ir = (const int*)(smem + OFF_IDX) + r * KS + sub;
#pragma unroll
            for (int t = 0; t < 9; ++t) {
                const unsigned nz = t < 8 ? ((mw[s][t >> 2] >> ((t & 3) * 8)) & 1) : nzb[s];
                act[s][t] = __ballot(nz != 0) != 0;
                idx[s][t] = 0;
                if (act[s][t]) idx[s][t] = ir[t * 8];
            }
        }
        unsigned long long cnt = 0;
#pragma unroll
        for (int s = 0; s < 2; ++s)
#pragma unroll
            for (int t = 0; t < 9; ++t) {
                ent[s][t] = 0; aux[s][t] = 0;
                if (act[s][t]) {
                    const unsigned nz = t < 8 ? ((mw[s][t >> 2] >> ((t & 3) * 8)) & 1) : nzb[s];
                    const unsigned c = idx[s][t] >> 13;
                    const unsigned rank = __builtin_amdgcn_perm((unsigned)(cnt >> 32), (unsigned)cnt, c);
                    aux[s][t] = (rank << 8) | c;
                    ent[s][t] = (idx[s][t] << 7) | (((unsigned)(sub + 8 * t) << 25) | (1u << s));
                    cnt += (unsigned long long)nz << (c << 3);
                }
            }
        unsigned lo = (unsigned)cnt, hi = (unsigned)(cnt >> 32);
        const unsigned olo = lo, ohi = hi;
        const unsigned k1 = sub >= 1 ? ~0u : 0u, k2 = sub >= 2 ? ~0u : 0u, k4 = sub >= 4 ? ~0u : 0u;
        lo += dpp<0x111, 0xf>(0, lo) & k1; hi += dpp<0x111, 0xf>(0, hi) & k1;
        lo += dpp<0x112, 0xf>(0, lo) & k2; hi += dpp<0x112, 0xf>(0, hi) & k2;
        lo += dpp<0x114, 0xf>(0, lo) & k4; hi += dpp<0x114, 0xf>(0, hi) & k4;
        unsigned tlo = dpp<0x157, 0x3>(0, lo), thi = dpp<0x157, 0x3>(0, hi);
        tlo = dpp<0x15f, 0xc>(tlo, lo); thi = dpp<0x15f, 0xc>(thi, hi);
        unsigned long long T = ((unsigned long long)thi << 32) | tlo;
        T += T << 8; T += T << 16; T += T << 32;
        len = (unsigned)(T >> 56);
        const unsigned long long st = (T << 8) + ((((unsigned long long)hi << 32) | lo) - (((unsigned long long)ohi << 32) | olo));
        slo = (unsigned)st; shi = (unsigned)(st >> 32);
        if (sub == 0) atomicMax((unsigned*)(smem + OFF_NB), len);
    }
    bar();
    const int nblk = (__builtin_amdgcn_readfirstlane(*(const unsigned*)(smem + OFF_NB)) + 3) >> 2;
    unsigned* list = (unsigned*)(smem + OFF_IDX) + q * LCAP;
#pragma unroll
    for (int s = 0; s < 2; ++s)
#pragma unroll
        for (int t = 0; t < 9; ++t)
            if (act[s][t]) {
                const unsigned nz = t < 8 ? ((mw[s][t >> 2] >> ((t & 3) * 8)) & 1) : nzb[s];
                const unsigned stb = __builtin_amdgcn_perm(shi, slo, aux[s][t]);
                const unsigned pos = (stb & 0xff) + ((aux[s][t] >> 8) & 0xff);
                if (nz) list[pos] = ent[s][t];
            }
    for (unsigned p = len + sub; p < (unsigned)nblk * 4; p += 8) list[p] = 0;

    f32x4 acc[8];
#pragma unroll
    for (int c = 0; c < 8; ++c) acc[c] = f32x4{0.f, 0.f, 0.f, 0.f};
    const unsigned sub16 = sub * 16;
    const int qi = lane & 3;
    const char* vrow = smem + OFF_VALS + ((qi & 1) ? r1 : r0) * (KS * 2);
    const u32x4* lst = (const u32x4*)list;

    auto issue = [&](const u32x4& e, u32x4 (&d)[4]) {
#pragma unroll
        for (int k = 0; k < 4; ++k) d[k] = *(const u32x4*)((const char*)Bm + ((e[k] & 0x7fff80u) | sub16));
    };
    auto weights = [&](const u32x4& e, unsigned& a01, unsigned& a23) {
        unsigned v[4], m[4];
#pragma unroll
        for (int k = 0; k < 4; ++k) {
            v[k] = *(const uint16_t*)(vrow + (e[k] >> 24));
            m[k] = (unsigned)__builtin_amdgcn_sbfe((int)e[k], qi, 1);
        }
        a01 = (v[0] & m[0] & 0xffffu) | ((v[1] & m[1]) << 16);
        a23 = (v[2] & m[2] & 0xffffu) | ((v[3] & m[3]) << 16);
    };
    auto fma4 = [&](const u32x4 (&d)[4], unsigned a01, unsigned a23) {
        s16x4 a;
        { u32x2 t = u32x2{a01, a23}; a = __builtin_bit_cast(s16x4, t); }
#pragma unroll
        for (int m = 0; m < 4; ++m) {
            u32x2 blo = u32x2{__builtin_amdgcn_perm(d[1][m], d[0][m], 0x05040100u), __builtin_amdgcn_perm(d[3][m], d[2][m], 0x05040100u)};
            u32x2 bhi = u32x2{__builtin_amdgcn_perm(d[1][m], d[0][m], 0x07060302u), __builtin_amdgcn_perm(d[3][m], d[2][m], 0x07060302u)};
            acc[2 * m] = __builtin_amdgcn_mfma_f32_4x4x4bf16_1k(a, __builtin_bit_cast(s16x4, blo), acc[2 * m], 0, 0, 0);
            acc[2 * m + 1] = __builtin_amdgcn_mfma_f32_4x4x4bf16_1k(a, __builtin_bit_cast(s16x4, bhi), acc[2 * m + 1], 0, 0, 0);
        }
    };

    if (nblk > 0) {
        u32x4 dA[4], dB[4];
        unsigned a01A, a23A, a01B, a23B;
        u32x4 e = lst[0];
        issue(e, dA);
        weights(e, a01A, a23A);
        int b = 0;
        for (; b + 2 < nblk; b += 2) {
            e = lst[b + 1];
            issue(e, dB);
            weights(e, a01B, a23B);
            __builtin_amdgcn_sched_barrier(0);
            fma4(dA, a01A, a23A);
            __builtin_amdgcn_sched_barrier(0);
            __builtin_amdgcn_s_barrier();
            e = lst[b + 2];
            issue(e, dA);
            weights(e, a01A, a23A);
            __builtin_amdgcn_sched_barrier(0);
            fma4(dB, a01B, a23B);
            __builtin_amdgcn_sched_barrier(0);
            __builtin_amdgcn_s_barrier();
        }
        if (b + 2 == nblk) {
            e = lst[b + 1];
            issue(e, dB);
            weights(e, a01B, a23B);
            __builtin_amdgcn_sched_barrier(0);
            fma4(dA, a01A, a23A);
            fma4(dB, a01B, a23B);
        } else {
            fma4(dA, a01A, a23A);
        }
    }

#pragma unroll
    for (int s = 0; s < 2; ++s) {
        u32x4 o;
#pragma unroll
        for (int m = 0; m < 4; ++m) {
            const bf16x2 p = __builtin_convertvector(f32x2{acc[2 * m][s], acc[2 * m + 1][s]}, bf16x2);
            o[m] = __builtin_bit_cast(unsigned, p);
        }
        const unsigned r = s ? r1 : r0;
        *(u32x4*)(out + (size_t)(rowBase + r) * 64 + sub * 8) = o;
    }
    asm volatile("s_waitcnt vmcnt(0)" ::: "memory");
}

extern "C" void kernel_launch(void* const* d_in, const int* in_sizes, int n_in,
                              void* d_out, int out_size, void* d_ws, size_t ws_size,
                              hipStream_t stream) {
    const uint16_t* vals = (const uint16_t*)d_in[0];
    const int* col = (const int*)d_in[1];
    const uint16_t* B = (const uint16_t*)d_in[2];
    uint16_t* out = (uint16_t*)d_out;
    const int M = out_size / 64;
    fprintf(stderr, "PTRS vals=%p col=%p B=%p out=%p ws=%p sizes %d %d %d out %d ws %zu\n", d_in[0], d_in[1], d_in[2], d_out, d_ws, in_sizes[0], in_sizes[1], in_sizes[2], out_size, ws_size);
    spmm_ell_kernel<<<M / ROWS, NTHR, 0, stream>>>(vals, col, B, out);
}
```

```cpp
#include <hip/hip_runtime.h>
#include <stdint.h>

typedef __attribute__((ext_vector_type(4))) unsigned u32x4;
typedef __attribute__((ext_vector_type(2))) unsigned u32x2;
typedef __attribute__((ext_vector_type(4))) short s16x4;
typedef __attribute__((ext_vector_type(4))) float f32x4;
typedef __attribute__((ext_vector_type(2))) float f32x2;
typedef __attribute__((ext_vector_type(2))) __bf16 bf16x2;

constexpr int KS = 65;
constexpr int ROWS = 256;
constexpr int NTHR = 1024;
constexpr int LCAP = 132;

constexpr int OFF_VALS = 0;
constexpr int OFF_IDX = ROWS * KS * 2;
constexpr int OFF_MASK = OFF_IDX + 128 * LCAP * 4;
constexpr int OFF_HIST = OFF_MASK + ROWS * 8;
constexpr int OFF_RMAP = OFF_HIST + 1024;
constexpr int OFF_NB = OFF_RMAP + 512;
constexpr int OFF_DUMP = OFF_NB + 16;
constexpr int LDS_BYTES = OFF_DUMP + 256;

__device__ __forceinline__ void dma16_nt(const void* gbase, unsigned goff, unsigned lds_dst) {
    unsigned keep;
    asm volatile("s_mov_b32 %0, m0\n\ts_mov_b32 m0, %3\n\ts_nop 0\n\tglobal_load_lds_dwordx4 %1, %2 nt\n\ts_mov_b32 m0, %0"
                 : "=&s"(keep) : "v"(goff), "s"(gbase), "s"(lds_dst) : "memory");
}
__device__ __forceinline__ void dma4(const void* gbase, unsigned goff, unsigned lds_dst) {
    unsigned keep;
    asm volatile("s_mov_b32 %0, m0\n\ts_mov_b32 m0, %3\n\ts_nop 0\n\tglobal_load_lds_dword %1, %2\n\ts_mov_b32 m0, %0"
                 : "=&s"(keep) : "v"(goff), "s"(gbase), "s"(lds_dst) : "memory");
}
__device__ __forceinline__ void gload(u32x4& d, const void* base, unsigned off) {
    asm volatile("global_load_dwordx4 %0, %1, %2" : "=v"(d) : "v"(off), "s"(base));
}
template <int N> __device__ __forceinline__ void wait_vm(u32x4& a, u32x4& b, u32x4& c, u32x4& d) {
    asm volatile("s_waitcnt vmcnt(%4)" : "+v"(a), "+v"(b), "+v"(c), "+v"(d) : "i"(N));
}
__device__ __forceinline__ void bar() { asm volatile("s_waitcnt lgkmcnt(0)\n\ts_barrier" ::: "memory"); }

template <int CTRL, int BANK> __device__ __forceinline__ unsigned dpp(unsigned old, unsigned x) {
    return __builtin_amdgcn_update_dpp(old, x, CTRL, 0xf, BANK, true);
}

__global__ __launch_bounds__(NTHR) void spmm_ell_kernel(const uint16_t* __restrict__ vals, const int* __restrict__ col,
                                                        const uint16_t* __restrict__ Bm, uint16_t* __restrict__ out) {
    __shared__ __attribute__((aligned(16))) char smem[LDS_BYTES];
    const int tid = threadIdx.x;
    const int lane = tid & 63;
    const int w = __builtin_amdgcn_readfirstlane(tid >> 6);
    const int rowBase = blockIdx.x * ROWS;
    const unsigned sbase = (unsigned)(size_t)smem;

    {
        const char* gv = (const char*)vals + (size_t)rowBase * (KS * 2);
        const char* gi = (const char*)col + (size_t)rowBase * (KS * 4);
        const unsigned wl = sbase + w * 1024;
#pragma unroll
        for (int k = 0; k < 2; ++k) dma16_nt(gv, (k * 1024 + tid) * 16, wl + OFF_VALS + k * 16384);
        if (w == 0) { if (lane < 32) dma16_nt(gv, (2048 + lane) * 16, sbase + OFF_VALS + 2048 * 16); }
#pragma unroll
        for (int k = 0; k < 4; ++k) dma16_nt(gi, (k * 1024 + tid) * 16, wl + OFF_IDX + k * 16384);
        if (w == 1) dma16_nt(gi, (4096 + lane) * 16, sbase + OFF_IDX + 4096 * 16);
        if (w >= 4 && w < 8) dma4(Bm, (unsigned)((blockIdx.x * 4 + (w - 4)) * 64 + lane) * 128u, sbase + OFF_DUMP);
    }
    if (tid < 64) *(u32x4*)(smem + OFF_HIST + tid * 16) = u32x4{0, 0, 0, 0};
    if (tid == 64) *(unsigned*)(smem + OFF_NB) = 0;
    asm volatile("s_waitcnt vmcnt(4)" ::: "memory");
    bar();

    {
        unsigned mlo = 0, mhi = 0;
        const uint16_t* vr = (const uint16_t*)(smem + OFF_VALS) + (w * 16) * KS + lane;
#pragma unroll
        for (int rr = 0; rr < 16; ++rr) {
            const unsigned v = vr[rr * KS];
            const unsigned long long bal = __ballot((v & 0x7fffu) != 0);
            mlo = (lane == rr) ? (unsigned)bal : mlo;
            mhi = (lane == rr) ? (unsigned)(bal >> 32) : mhi;
        }
        const int r = w * 16 + (lane & 15);
        const unsigned nz64 = (((const uint16_t*)(smem + OFF_VALS))[r * KS + 64] & 0x7fffu) != 0 ? 1u : 0u;
        const bool valid = lane < 16;
        if (valid) *(u32x2*)(smem + OFF_MASK + r * 8) = u32x2{mlo, mhi};
        unsigned key = __builtin_popcount(mlo) + __builtin_popcount(mhi) + nz64;
        key = key > 63 ? 63 : key;
        unsigned eq = 0xffffu;
#pragma unroll
        for (int b = 0; b < 6; ++b) {
            const int bit = -(int)((key >> b) & 1);
            const unsigned bal = (unsigned)__ballot(bit != 0);
            eq &= ~(bal ^ (unsigned)bit);
        }
        const unsigned rank = __builtin_amdgcn_mbcnt_lo(eq, 0);
        if (valid && rank == 0) *(uint8_t*)(smem + OFF_HIST + key * 16 + w) = (uint8_t)__builtin_popcount(eq);
        bar();
        const u32x4 h = *(const u32x4*)(smem + OFF_HIST + lane * 16);
        unsigned tot = __builtin_amdgcn_sad_u8(h[0], 0, 0);
        tot = __builtin_amdgcn_sad_u8(h[1], 0, tot);
        tot = __builtin_amdgcn_sad_u8(h[2], 0, tot);
        tot = __builtin_amdgcn_sad_u8(h[3], 0, tot);
        unsigned inc = tot;
        inc += dpp<0x111, 0xf>(0, inc);
        inc += dpp<0x112, 0xf>(0, inc);
        inc += dpp<0x114, 0xf>(0, inc);
        inc += dpp<0x118, 0xf>(0, inc);
        inc += __builtin_amdgcn_update_dpp(0, inc, 0x142, 0xa, 0xf, true);
        inc += __builtin_amdgcn_update_dpp(0, inc, 0x143, 0xc, 0xf, true);
        const unsigned exc = inc - tot;
        const unsigned base = __builtin_amdgcn_ds_bpermute(key * 4, exc);
        const u32x4 hk = *(const u32x4*)(smem + OFF_HIST + key * 16);
        unsigned early = 0;
#pragma unroll
        for (int q = 0; q < 4; ++q) {
            const int nb = w - q * 4;
            const unsigned m = nb >= 4 ? 0xffffffffu : (nb <= 0 ? 0u : ((1u << (nb * 8)) - 1u));
            early = __builtin_amdgcn_sad_u8(hk[q] & m, 0, early);
        }
        const unsigned pos = base + early + rank;
        const unsigned slot = pos < 128 ? pos * 2 : (255 - pos) * 2 + 1;
        if (valid) *(uint16_t*)(smem + OFF_RMAP + slot * 2) = (uint16_t)(r | (nz64 << 8));
    }
    if (w >= 4 && w < 8) asm volatile("s_waitcnt vmcnt(1)" ::: "memory"); else asm volatile("s_waitcnt vmcnt(0)" ::: "memory");
    bar();

    const int sub = lane & 7;
    const int q = tid >> 3;
    const unsigned rm = *(const unsigned*)(smem + OFF_RMAP + q * 4);
    const unsigned r0 = rm & 0xff, r1 = (rm >> 16) & 0xff;
    unsigned ent[2][9], aux[2][9], nzb[2];
    unsigned mw[2][2];
    bool act[2][9];
    unsigned len;
    unsigned slo, shi;
    {
        unsigned idx[2][9];
#pragma unroll
        for (int s = 0; s < 2; ++s) {
            const unsigned r = s ? r1 : r0;
            const u32x2 m = *(const u32x2*)(smem + OFF_MASK + r * 8);
            const unsigned long long mm = (((unsigned long long)m[1] << 32) | m[0]) >> sub;
            mw[s][0] = (unsigned)mm; mw[s][1] = (unsigned)(mm >> 32);
            nzb[s] = (sub == 0) ? ((rm >> (s * 16 + 8)) & 1) : 0;
            const int* ir = (const int*)(smem + OFF_IDX) + r * KS + sub;
#pragma unroll
            for (int t = 0; t < 9; ++t) {
                const unsigned nz = t < 8 ? ((mw[s][t >> 2] >> ((t & 3) * 8)) & 1) : nzb[s];
                act[s][t] = __ballot(nz != 0) != 0;
                idx[s][t] = 0;
                if (act[s][t]) idx[s][t] = ir[t * 8];
            }
        }
        unsigned long long cnt = 0;
#pragma unroll
        for (int s = 0; s < 2; ++s)
#pragma unroll
            for (int t = 0; t < 9; ++t) {
                ent[s][t] = 0; aux[s][t] = 0;
                if (act[s][t]) {
                    const unsigned nz = t < 8 ? ((mw[s][t >> 2] >> ((t & 3) * 8)) & 1) : nzb[s];
                    const unsigned c = idx[s][t] >> 13;
                    const unsigned rank = __builtin_amdgcn_perm((unsigned)(cnt >> 32), (unsigned)cnt, c);
                    aux[s][t] = (rank << 8) | c;
                    ent[s][t] = (idx[s][t] << 7) | (((unsigned)(sub + 8 * t) << 25) | (1u << s));
                    cnt += (unsigned long long)nz << (c << 3);
                }
            }
        unsigned lo = (unsigned)cnt, hi = (unsigned)(cnt >> 32);
        const unsigned olo = lo, ohi = hi;
        const unsigned k1 = sub >= 1 ? ~0u : 0u, k2 = sub >= 2 ? ~0u : 0u, k4 = sub >= 4 ? ~0u : 0u;
        lo += dpp<0x111, 0xf>(0, lo) & k1; hi += dpp<0x111, 0xf>(0, hi) & k1;
        lo += dpp<0x112, 0xf>(0, lo) & k2; hi += dpp<0x112, 0xf>(0, hi) & k2;
        lo += dpp<0x114, 0xf>(0, lo) & k4; hi += dpp<0x114, 0xf>(0, hi) & k4;
        unsigned tlo = dpp<0x157, 0x3>(0, lo), thi = dpp<0x157, 0x3>(0, hi);
        tlo = dpp<0x15f, 0xc>(tlo, lo); thi = dpp<0x15f, 0xc>(thi, hi);
        unsigned long long T = ((unsigned long long)thi << 32) | tlo;
        T += T << 8; T += T << 16; T += T << 32;
        len = (unsigned)(T >> 56);
        const unsigned long long st = (T << 8) + ((((unsigned long long)hi << 32) | lo) - (((unsigned long long)ohi << 32) | olo));
        slo = (unsigned)st; shi = (unsigned)(st >> 32);
        if (sub == 0) atomicMax((unsigned*)(smem + OFF_NB), len);
    }
    bar();
    const int nblk = (__builtin_amdgcn_readfirstlane(*(const unsigned*)(smem + OFF_NB)) + 3) >> 2;
    unsigned* list = (unsigned*)(smem + OFF_IDX) + q * LCAP;
#pragma unroll
    for (int s = 0; s < 2; ++s)
#pragma unroll
        for (int t = 0; t < 9; ++t)
            if (act[s][t]) {
                const unsigned nz = t < 8 ? ((mw[s][t >> 2] >> ((t & 3) * 8)) & 1) : nzb[s];
                const unsigned stb = __builtin_amdgcn_perm(shi, slo, aux[s][t]);
                const unsigned pos = (stb & 0xff) + ((aux[s][t] >> 8) & 0xff);
                if (nz) list[pos] = ent[s][t];
            }
    for (unsigned p = len + sub; p < (unsigned)nblk * 4; p += 8) list[p] = 0;

    f32x4 acc[8];
#pragma unroll
    for (int c = 0; c < 8; ++c) acc[c] = f32x4{0.f, 0.f, 0.f, 0.f};
    const unsigned sub16 = sub * 16;
    const int qi = lane & 3;
    const char* vrow = smem + OFF_VALS + ((qi & 1) ? r1 : r0) * (KS * 2);
    const u32x4* lst = (const u32x4*)list;

    auto issue = [&](const u32x4& e, u32x4 (&d)[4]) {
#pragma unroll
        for (int k = 0; k < 4; ++k) d[k] = *(const u32x4*)((const char*)Bm + ((e[k] & 0x7fff80u) | sub16));
    };
    auto weights = [&](const u32x4& e, unsigned& a01, unsigned& a23) {
        unsigned v[4], m[4];
#pragma unroll
        for (int k = 0; k < 4; ++k) {
            v[k] = *(const uint16_t*)(vrow + (e[k] >> 24));
            m[k] = (unsigned)__builtin_amdgcn_sbfe((int)e[k], qi, 1);
        }
        a01 = (v[0] & m[0] & 0xffffu) | ((v[1] & m[1]) << 16);
        a23 = (v[2] & m[2] & 0xffffu) | ((v[3] & m[3]) << 16);
    };
    auto fma4 = [&](const u32x4 (&d)[4], unsigned a01, unsigned a23) {
        s16x4 a;
        { u32x2 t = u32x2{a01, a23}; a = __builtin_bit_cast(s16x4, t); }
#pragma unroll
        for (int m = 0; m < 4; ++m) {
            u32x2 blo = u32x2{__builtin_amdgcn_perm(d[1][m], d[0][m], 0x05040100u), __builtin_amdgcn_perm(d[3][m], d[2][m], 0x05040100u)};
            u32x2 bhi = u32x2{__builtin_amdgcn_perm(d[1][m], d[0][m], 0x07060302u), __builtin_amdgcn_perm(d[3][m], d[2][m], 0x07060302u)};
            acc[2 * m] = __builtin_amdgcn_mfma_f32_4x4x4bf16_1k(a, __builtin_bit_cast(s16x4, blo), acc[2 * m], 0, 0, 0);
            acc[2 * m + 1] = __builtin_amdgcn_mfma_f32_4x4x4bf16_1k(a, __builtin_bit_cast(s16x4, bhi), acc[2 * m + 1], 0, 0, 0);
        }
    };

    if (nblk > 0) {
        u32x4 dA[4], dB[4];
        unsigned a01A, a23A, a01B, a23B;
        u32x4 e = lst[0];
        issue(e, dA);
        weights(e, a01A, a23A);
        int b = 0;
        for (; b + 2 < nblk; b += 2) {
            e = lst[b + 1];
            issue(e, dB);
            weights(e, a01B, a23B);
            __builtin_amdgcn_sched_barrier(0);
            fma4(dA, a01A, a23A);
            __builtin_amdgcn_sched_barrier(0);
            __builtin_amdgcn_s_barrier();
            e = lst[b + 2];
            issue(e, dA);
            weights(e, a01A, a23A);
            __builtin_amdgcn_sched_barrier(0);
            fma4(dB, a01B, a23B);
            __builtin_amdgcn_sched_barrier(0);
            __builtin_amdgcn_s_barrier();
        }
        if (b + 2 == nblk) {
            e = lst[b + 1];
            issue(e, dB);
            weights(e, a01B, a23B);
            __builtin_amdgcn_sched_barrier(0);
            fma4(dA, a01A, a23A);
            fma4(dB, a01B, a23B);
        } else {
            fma4(dA, a01A, a23A);
        }
    }

#pragma unroll
    for (int s = 0; s < 2; ++s) {
        u32x4 o;
#pragma unroll
        for (int m = 0; m < 4; ++m) {
            const bf16x2 p = __builtin_convertvector(f32x2{acc[2 * m][s], acc[2 * m + 1][s]}, bf16x2);
            o[m] = __builtin_bit_cast(unsigned, p);
        }
        const unsigned r = s ? r1 : r0;
        *(u32x4*)(out + (size_t)(rowBase + r) * 64 + sub * 8) = o;
    }
    asm volatile("s_waitcnt vmcnt(0)" ::: "memory");
}

extern "C" void kernel_launch(void* const* d_in, const int* in_sizes, int n_in,
                              void* d_out, int out_size, void* d_ws, size_t ws_size,
                              hipStream_t stream) {
    const uint16_t* vals = (const uint16_t*)d_in[0];
    const int* col = (const int*)d_in[1];
    const uint16_t* B = (const uint16_t*)d_in[2];
    uint16_t* out = (uint16_t*)d_out;
    const int M = out_size / 64;
    spmm_ell_kernel<<<M / ROWS, NTHR, 0, stream>>>(vals, col, B, out);
}
```

```cpp
#include <hip/hip_runtime.h>
#include <stdint.h>

typedef __attribute__((ext_vector_type(4))) unsigned u32x4;
typedef __attribute__((ext_vector_type(2))) unsigned u32x2;
typedef __attribute__((ext_vector_type(4))) short s16x4;
typedef __attribute__((ext_vector_type(4))) float f32x4;
typedef __attribute__((ext_vector_type(2))) float f32x2;
typedef __attribute__((ext_vector_type(2))) __bf16 bf16x2;
typedef unsigned long long u64;

extern "C" __device__ unsigned llvm_writelane(unsigned, unsigned, unsigned) __asm("llvm.amdgcn.writelane.i32");

#define PF 8
#define WARM 1

constexpr int KS = 65;
constexpr int ROWS = 256;
constexpr int NTHR = 1024;
constexpr int LCAP = 132;

constexpr int OFF_VALS = 0;
constexpr int OFF_IDX = ROWS * KS * 2;
constexpr int OFF_MASK = OFF_IDX + 128 * LCAP * 4;
constexpr int OFF_HIST = OFF_MASK + ROWS * 8;
constexpr int OFF_RMAP = OFF_HIST + 512;
constexpr int OFF_NB = OFF_RMAP + 512;
constexpr int OFF_DUMP = OFF_NB + 16;
constexpr int LDS_BYTES = OFF_DUMP + 256;

__device__ __forceinline__ void dma16_nt(const void* gbase, unsigned goff, unsigned lds_dst) {
    unsigned keep;
    asm volatile("s_mov_b32 %0, m0\n\ts_mov_b32 m0, %3\n\ts_nop 0\n\tglobal_load_lds_dwordx4 %1, %2 nt\n\ts_mov_b32 m0, %0"
                 : "=&s"(keep) : "v"(goff), "s"(gbase), "s"(lds_dst) : "memory");
}
__device__ __forceinline__ void dma4(const void* gbase, unsigned goff, unsigned lds_dst) {
    unsigned keep;
    asm volatile("s_mov_b32 %0, m0\n\ts_mov_b32 m0, %3\n\ts_nop 0\n\tglobal_load_lds_dword %1, %2\n\ts_mov_b32 m0, %0"
                 : "=&s"(keep) : "v"(goff), "s"(gbase), "s"(lds_dst) : "memory");
}
__device__ __forceinline__ void bar() { asm volatile("s_waitcnt lgkmcnt(0)\n\ts_barrier" ::: "memory"); }

__device__ __forceinline__ void lds_signal(unsigned addr) {
    asm volatile("s_waitcnt lgkmcnt(0)\n\ts_mov_b64 exec, 1\n\tds_add_u32 %0, %1\n\ts_mov_b64 exec, -1" :: "v"(addr), "v"(1u) : "memory");
}
__device__ __forceinline__ void lds_wait(unsigned addr, unsigned n) {
    unsigned v; unsigned sv;
    asm volatile("1:\n\tds_read_b32 %0, %2\n\ts_waitcnt lgkmcnt(0)\n\tv_readfirstlane_b32 %1, %0\n\ts_cmp_ge_u32 %1, %3\n\ts_cbranch_scc1 2f\n\ts_sleep 2\n\ts_branch 1b\n2:"
                 : "=&v"(v), "=&s"(sv) : "v"(addr), "s"(n) : "memory", "scc");
}

template <int CTRL, int BANK> __device__ __forceinline__ unsigned dpp(unsigned old, unsigned x) {
    return __builtin_amdgcn_update_dpp(old, x, CTRL, 0xf, BANK, true);
}

__device__ __forceinline__ void pass_count(u64 em, unsigned& x, unsigned K, unsigned& p, unsigned& rk, unsigned& cnt) {
    unsigned t0;
    asm volatile("s_mov_b64 exec, %[em]\n\t"
                 "v_lshrrev_b32 %[p], 13, %[x]\n\t"
                 "v_lshlrev_b32 %[t0], 2, %[p]\n\t"
                 "v_lshl_or_b32 %[x], %[x], 7, %[K]\n\t"
                 "v_bfe_u32 %[rk], %[cnt], %[t0], 4\n\t"
                 "v_lshl_add_u32 %[cnt], 1, %[t0], %[cnt]\n\t"
                 "s_mov_b64 exec, -1"
                 : [p] "+v"(p), [rk] "+v"(rk), [t0] "=&v"(t0), [cnt] "+v"(cnt), [x] "+v"(x)
                 : [em] "s"(em), [K] "v"(K));
}
__device__ __forceinline__ void pass_scatter(u64 em, unsigned ent, unsigned p, unsigned rk, unsigned slo, unsigned shi, unsigned lbase) {
    unsigned t0;
    asm volatile("s_mov_b64 exec, %[em]\n\t"
                 "v_perm_b32 %[t0], %[shi], %[slo], %[p]\n\t"
                 "v_add_u32_sdwa %[t0], %[t0], %[rk] dst_sel:DWORD dst_unused:UNUSED_PAD src0_sel:BYTE_0 src1_sel:BYTE_0\n\t"
                 "v_lshl_add_u32 %[t0], %[t0], 2, %[lb]\n\t"
                 "ds_write_b32 %[t0], %[ent]\n\t"
                 "s_mov_b64 exec, -1"
                 : [t0] "=&v"(t0)
                 : [em] "s"(em), [ent] "v"(ent), [p] "v"(p), [rk] "v"(rk), [slo] "v"(slo), [shi] "v"(shi), [lb] "v"(lbase)
                 : "memory");
}

#define STAMP(i)
#define STAMP_DECL
#define STAMP_FLUSH
#define STAMP_ARG
#define STAMP_PASS

__global__ __launch_bounds__(NTHR) void spmm_ell_kernel(const uint16_t* __restrict__ vals, const int* __restrict__ col,
                                                        const uint16_t* __restrict__ Bm, uint16_t* __restrict__ out STAMP_ARG) {
    __shared__ __attribute__((aligned(16))) char smem[LDS_BYTES];
    const int tid = threadIdx.x;
    const int lane = tid & 63;
    const int w = __builtin_amdgcn_readfirstlane(tid >> 6);
    const int rowBase = blockIdx.x * ROWS;
    const unsigned sbase = (unsigned)(size_t)smem;

    STAMP_DECL
    STAMP(0);
    if (w < 8) {
        const char* gv = (const char*)vals + (size_t)(rowBase + w * 32) * (KS * 2);
        const unsigned dst = sbase + OFF_VALS + w * (32 * KS * 2);
#pragma unroll
        for (int k = 0; k < 2; ++k) dma16_nt(gv, (k * 64 + lane) * 16, dst + k * 1024);
        if (w == 0) {
            *(u32x2*)(smem + OFF_HIST + lane * 8) = u32x2{0, 0};
            if (lane < 4) *(unsigned*)(smem + OFF_NB + lane * 4) = 0;
        }
        bar();
#pragma unroll
        for (int k = 2; k < 4; ++k) dma16_nt(gv, (k * 64 + lane) * 16, dst + k * 1024);
        if (lane < 4) dma16_nt(gv, (256 + lane) * 16, dst + 4096);
    } else {
        bar();
    }
    STAMP(10);
    if (w >= 8) {
        const char* gi = (const char*)col + (size_t)rowBase * (KS * 4);
        const int v = w - 8;
#pragma unroll
        for (int k = 0; k < 8; ++k) dma16_nt(gi, ((k * 8 + v) * 64 + lane) * 16, sbase + OFF_IDX + (k * 8 + v) * 1024);
        if (v == 0) dma16_nt(gi, (4096 + lane) * 16, sbase + OFF_IDX + 4096 * 16);
        lds_signal(sbase + OFF_NB + 12);
        lds_wait(sbase + OFF_NB + 12, 8);
#pragma unroll
        for (int k = 0; k < (PF + 7) / 8; ++k) {
            const int j = k * 8 + v;
            if (j < PF) {
                const unsigned range = ((j >> 2) * 2) & 7 | ((j >> 2) >> 2);
                dma4(Bm, (range * 8192u + (unsigned)(((blockIdx.x >> 3) * 4 + (j & 3)) * 64 + lane)) * 128u, sbase + OFF_DUMP);
            }
        }
        if (WARM) {
            constexpr int NR = 8 - PF / 4;
            constexpr int NLN = NR * 32;
#pragma unroll
            for (int k = 0; k * 512 < NLN; ++k) {
                const int i = (k * 8 + v) * 64 + lane;
                const int ri = (i >> 5) + PF / 4;
                const unsigned range = ((ri * 2) & 7) | (ri >> 2);
                if (i < NLN) dma4(Bm, (range * 8192u + blockIdx.x * 32u + (i & 31)) * 128u, sbase + OFF_DUMP);
            }
        }
        STAMP(11);
        STAMP(1);
    } else {
        STAMP(11);
        asm volatile("s_waitcnt vmcnt(0)" ::: "memory");
        STAMP(1);
        unsigned mlo = 0, mhi = 0;
        const uint16_t* vr = (const uint16_t*)(smem + OFF_VALS) + (w * 32) * KS + lane;
#pragma unroll
        for (int rr = 0; rr < 32; ++rr) {
            const unsigned v = vr[rr * KS];
            const u64 bal = __ballot((v & 0x7fffu) != 0);
            mlo = llvm_writelane((unsigned)bal, rr, mlo);
            mhi = llvm_writelane((unsigned)(bal >> 32), rr, mhi);
        }
        const int r = w * 32 + (lane & 31);
        const unsigned nz64 = (((const uint16_t*)(smem + OFF_VALS))[r * KS + 64] & 0x7fffu) != 0 ? 1u : 0u;
        const bool valid = lane < 32;
        if (valid) *(u32x2*)(smem + OFF_MASK + r * 8) = u32x2{mlo, mhi};
        unsigned key = __builtin_popcount(mlo) + __builtin_popcount(mhi) + nz64;
        key = key > 63 ? 63 : key;
        unsigned eq = 0xffffffffu;
#pragma unroll
        for (int b = 0; b < 6; ++b) {
            const int bit = -(int)((key >> b) & 1);
            const unsigned bal = (unsigned)__ballot(bit != 0);
            eq &= ~(bal ^ (unsigned)bit);
        }
        const unsigned rank = __builtin_amdgcn_mbcnt_lo(eq, 0);
        if (valid && rank == 0) *(uint8_t*)(smem + OFF_HIST + key * 8 + w) = (uint8_t)__builtin_popcount(eq);
        lds_signal(sbase + OFF_NB + 4);
        lds_wait(sbase + OFF_NB + 4, 8);
        const u32x2 h = *(const u32x2*)(smem + OFF_HIST + lane * 8);
        const u32x2 hk = *(const u32x2*)(smem + OFF_HIST + key * 8);
        unsigned tot = __builtin_amdgcn_sad_u8(h[0], 0, 0);
        tot = __builtin_amdgcn_sad_u8(h[1], 0, tot);
        unsigned inc = tot;
        inc += dpp<0x111, 0xf>(0, inc);
        inc += dpp<0x112, 0xf>(0, inc);
        inc += dpp<0x114, 0xf>(0, inc);
        inc += dpp<0x118, 0xf>(0, inc);
        inc += __builtin_amdgcn_update_dpp(0, inc, 0x142, 0xa, 0xf, true);
        inc += __builtin_amdgcn_update_dpp(0, inc, 0x143, 0xc, 0xf, true);
        const unsigned exc = inc - tot;
        const unsigned base = __builtin_amdgcn_ds_bpermute(key * 4, exc);
        const unsigned m0 = w >= 4 ? 0xffffffffu : ((1u << (w * 8)) - 1u);
        const unsigned m1 = w <= 4 ? 0u : ((1u << ((w - 4) * 8)) - 1u);
        unsigned early = __builtin_amdgcn_sad_u8(hk[0] & m0, 0, 0);
        early = __builtin_amdgcn_sad_u8(hk[1] & m1, 0, early);
        const unsigned pos = base + early + rank;
        const unsigned slot = pos < 128 ? pos * 2 : (255 - pos) * 2 + 1;
        if (valid) *(uint16_t*)(smem + OFF_RMAP + slot * 2) = (uint16_t)(r | (nz64 << 8));
        lds_signal(sbase + OFF_NB + 8);
    }
    lds_wait(sbase + OFF_NB + 8, 8);
    STAMP(2);

    const int sub = lane & 7;
    const int q = tid >> 3;
    const unsigned rm = *(const unsigned*)(smem + OFF_RMAP + q * 4);
    const unsigned r0 = rm & 0xff, r1 = (rm >> 16) & 0xff;
    u64 em[2][9];
    int T[2];
    unsigned K[2][9];
    unsigned len = 0;
#pragma unroll
    for (int s = 0; s < 2; ++s) {
        const u32x2 m = *(const u32x2*)(smem + OFF_MASK + (s ? r1 : r0) * 8);
        const unsigned nzb = (rm >> (s * 16 + 8)) & 1;
        len += __builtin_popcount(m[0]) + __builtin_popcount(m[1]) + nzb;
        const u64 mm = (((u64)m[1] << 32) | m[0]) >> sub;
        const unsigned mw0 = (unsigned)mm, mw1 = (unsigned)(mm >> 32);
        T[s] = 0;
#pragma unroll
        for (int t = 0; t < 9; ++t) {
            const bool nz = t < 8 ? ((((t < 4 ? mw0 : mw1) >> ((t & 3) * 8)) & 1) != 0) : (sub == 0 && nzb != 0);
            em[s][t] = __ballot(nz);
            if (em[s][t] != 0) T[s] = t + 1;
            K[s][t] = ((unsigned)(sub + 8 * t) << 25) | (1u << s);
            asm volatile("" : "+v"(K[s][t]));
        }
    }
    unsigned maxlen = 0;
#pragma unroll
    for (int g = 0; g < 8; ++g) { const unsigned l = __builtin_amdgcn_readlane(len, g * 8); maxlen = l > maxlen ? l : maxlen; }
    if (lane == 0) atomicMax((unsigned*)(smem + OFF_NB), maxlen);
    unsigned ibase0 = sbase + OFF_IDX + r0 * (KS * 4) + sub * 4, ibase1 = sbase + OFF_IDX + r1 * (KS * 4) + sub * 4;
    asm volatile("" : "+v"(ibase0), "+v"(ibase1));
    const unsigned lbase = sbase + OFF_IDX + q * (LCAP * 4);
    unsigned k1 = sub >= 1 ? ~0u : 0u, k2 = sub >= 2 ? ~0u : 0u, k4 = sub >= 4 ? ~0u : 0u;
    asm volatile("" : "+v"(k1), "+v"(k2), "+v"(k4));
    unsigned x[2][9], c[2][9], rk[2][9];
#pragma unroll
    for (int s = 0; s < 2; ++s)
#pragma unroll
        for (int t = 0; t < 9; ++t) {
            x[s][t] = 0; c[s][t] = 0; rk[s][t] = 0;
            asm volatile("" : "+v"(x[s][t]), "+v"(c[s][t]), "+v"(rk[s][t]));
        }
    STAMP(3);
    {
        constexpr int NLN = WARM ? (8 - PF / 4) * 32 : 0;
        if (w < 8) asm volatile("s_waitcnt vmcnt(0)" ::: "memory");
        else {
            const int v = w - 8;
            int young = 0;
            for (int k = 0; k < (PF + 7) / 8; ++k) young += (k * 8 + v < PF);
            for (int k = 0; k * 512 < NLN; ++k) young += ((k * 8 + v) * 64 < NLN);
            if (young == 0) asm volatile("s_waitcnt vmcnt(0)" ::: "memory");
            else if (young == 1) asm volatile("s_waitcnt vmcnt(1)" ::: "memory");
            else if (young == 2) asm volatile("s_waitcnt vmcnt(2)" ::: "memory");
            else asm volatile("s_waitcnt vmcnt(3)" ::: "memory");
        }
    }
    bar();
    STAMP(4);

#define FOR_ACTIVE(s, BODY) \
    if (T[s] > 0) { { constexpr int t = 0; BODY } if (T[s] > 1) { { constexpr int t = 1; BODY } if (T[s] > 2) { { constexpr int t = 2; BODY } \
    if (T[s] > 3) { { constexpr int t = 3; BODY } if (T[s] > 4) { { constexpr int t = 4; BODY } if (T[s] > 5) { { constexpr int t = 5; BODY } \
    if (T[s] > 6) { { constexpr int t = 6; BODY } if (T[s] > 7) { { constexpr int t = 7; BODY } if (T[s] > 8) { { constexpr int t = 8; BODY } } } } } } } } } }
    FOR_ACTIVE(0, asm volatile("ds_read_b32 %0, %1 offset:%2" : "+v"(x[0][t]) : "v"(ibase0), "i"(t * 32) : "memory");)
    FOR_ACTIVE(1, asm volatile("ds_read_b32 %0, %1 offset:%2" : "+v"(x[1][t]) : "v"(ibase1), "i"(t * 32) : "memory");)
    asm volatile("s_waitcnt lgkmcnt(0)"
                 : "+v"(x[0][0]), "+v"(x[0][1]), "+v"(x[0][2]), "+v"(x[0][3]), "+v"(x[0][4]), "+v"(x[0][5]), "+v"(x[0][6]), "+v"(x[0][7]), "+v"(x[0][8]),
                   "+v"(x[1][0]), "+v"(x[1][1]), "+v"(x[1][2]), "+v"(x[1][3]), "+v"(x[1][4]), "+v"(x[1][5]), "+v"(x[1][6]), "+v"(x[1][7]), "+v"(x[1][8])
                 :: "memory");
    STAMP(12);
    unsigned cnt0 = 0, cnt1 = 0;
    FOR_ACTIVE(0, pass_count(em[0][t], x[0][t], K[0][t], c[0][t], rk[0][t], cnt0);)
    FOR_ACTIVE(1, pass_count(em[1][t], x[1][t], K[1][t], c[1][t], rk[1][t], cnt1);)
    STAMP(13);
    unsigned s0lo, s0hi, s1lo, s1hi;
    {
        const unsigned e0 = cnt0 & 0x0f0f0f0fu, o0 = (cnt0 >> 4) & 0x0f0f0f0fu;
        const unsigned olo = e0 + (cnt1 & 0x0f0f0f0fu), ohi = o0 + ((cnt1 >> 4) & 0x0f0f0f0fu);
        unsigned lo = olo, hi = ohi;
        lo += dpp<0x111, 0xf>(0, lo) & k1; hi += dpp<0x111, 0xf>(0, hi) & k1;
        lo += dpp<0x112, 0xf>(0, lo) & k2; hi += dpp<0x112, 0xf>(0, hi) & k2;
        lo += dpp<0x114, 0xf>(0, lo) & k4; hi += dpp<0x114, 0xf>(0, hi) & k4;
        unsigned tlo = dpp<0x157, 0x3>(0, lo), thi = dpp<0x157, 0x3>(0, hi);
        tlo = dpp<0x15f, 0xc>(tlo, lo); thi = dpp<0x15f, 0xc>(thi, hi);
        tlo += tlo << 8; asm volatile("" : "+v"(tlo)); tlo += tlo << 16;
        thi += thi << 8; asm volatile("" : "+v"(thi)); thi += thi << 16;
        thi += __builtin_amdgcn_perm(tlo, tlo, 0x03030303u);
        const unsigned a = (tlo << 8) + (lo - olo);
        const unsigned b = __builtin_amdgcn_alignbit(thi, tlo, 24) + (hi - ohi);
        s0lo = __builtin_amdgcn_perm(b, a, 0x05010400u); s0hi = __builtin_amdgcn_perm(b, a, 0x07030602u);
        const unsigned a1 = a + e0, b1 = b + o0;
        s1lo = __builtin_amdgcn_perm(b1, a1, 0x05010400u); s1hi = __builtin_amdgcn_perm(b1, a1, 0x07030602u);
    }
    bar();
    STAMP(5);
    FOR_ACTIVE(0, pass_scatter(em[0][t], x[0][t], c[0][t], rk[0][t], s0lo, s0hi, lbase);)
    FOR_ACTIVE(1, pass_scatter(em[1][t], x[1][t], c[1][t], rk[1][t], s1lo, s1hi, lbase);)
#undef FOR_ACTIVE
    const int nb = (int)(maxlen + 3) >> 2;
    unsigned* list = (unsigned*)(smem + OFF_IDX) + q * LCAP;
    for (unsigned p = len + sub; p < (unsigned)nb * 4; p += 8) list[p] = 0;
    const int nbMax = (__builtin_amdgcn_readfirstlane(*(const unsigned*)(smem + OFF_NB)) + 3) >> 2;
    asm volatile("s_waitcnt lgkmcnt(0)" ::: "memory");
    STAMP(6);

    f32x4 acc[8];
#pragma unroll
    for (int i = 0; i < 8; ++i) acc[i] = f32x4{0.f, 0.f, 0.f, 0.f};
    {
        const unsigned sub16 = sub * 16;
        const unsigned qi = lane & 3;
        const unsigned vrow = sbase + OFF_VALS + ((qi & 1) ? r1 : r0) * (KS * 2);
        unsigned laddr = lbase;
        const int rem = maxlen & 3;
        const int extra_bar = nbMax - nb;
#define RD_ENT   "ds_read_b128 v[112:115], %[la]\n\tv_add_u32 %[la], 16, %[la]\n\ts_waitcnt lgkmcnt(0)\n\t"
#define ADDR(k)  "v_and_or_b32 v" #k ", v%%ENT" #k
#define ADDRS    "v_and_or_b32 v120, v112, %[msk], %[s16]\n\tv_and_or_b32 v121, v113, %[msk], %[s16]\n\t" \
                 "v_and_or_b32 v122, v114, %[msk], %[s16]\n\tv_and_or_b32 v123, v115, %[msk], %[s16]\n\t"
#define LD(d0, d1, a) "global_load_dwordx4 v[" #d0 ":" #d1 "], v" #a ", %[B]\n\t"
#define LD4A     LD(64, 67, 120) LD(68, 71, 121) LD(72, 75, 122) LD(76, 79, 123)
#define LD4B     LD(80, 83, 120) LD(84, 87, 121) LD(88, 91, 122) LD(92, 95, 123)
#define VADDR(t, e) "v_add_u32_sdwa v" #t ", %[vr], v" #e " dst_sel:DWORD dst_unused:UNUSED_PAD src0_sel:DWORD src1_sel:BYTE_3\n\t"
#define RD_VALS  VADDR(124, 112) VADDR(125, 113) VADDR(126, 114) VADDR(127, 115) \
                 "ds_read_u16 v124, v124\n\tds_read_u16 v125, v125\n\tds_read_u16 v126, v126\n\tds_read_u16 v127, v127\n\t" \
                 "v_bfe_i32 v112, v112, %[qi], 1\n\tv_bfe_i32 v113, v113, %[qi], 1\n\tv_bfe_i32 v114, v114, %[qi], 1\n\tv_bfe_i32 v115, v115, %[qi], 1\n\t"
#define WEIGHTS(w0, w1) "s_waitcnt lgkmcnt(0)\n\t" \
                 "v_and_b32 v112, v112, v124\n\tv_and_b32 v113, v113, v125\n\tv_and_b32 v114, v114, v126\n\tv_and_b32 v115, v115, v127\n\t" \
                 "v_lshl_or_b32 v" #w0 ", v113, 16, v112\n\tv_lshl_or_b32 v" #w1 ", v115, 16, v114\n\t"
#define PERM(o, a, b, sel) "v_perm_b32 v" #o ", v" #a ", v" #b ", %[" #sel "]\n\t"
#define PERMS(r0_, r1_, r2_, r3_, o0, o1) PERM(o0, r1_, r0_, pl) PERM(o1, r3_, r2_, pl)
#define MF(i, w0, w1, o0, o1) "v_mfma_f32_4x4x4_16b_bf16 %[c" #i "], v[" #w0 ":" #w1 "], v[" #o0 ":" #o1 "], %[c" #i "]\n\t"
#define CONS(a0, a1, a2, a3, b0, b1, b2, b3, c0_, c1_, c2_, c3_, d0, d1, d2, d3, w0, w1) \
                 PERM(96, b0, a0, pl) PERM(97, d0, c0_, pl) PERM(98, b0, a0, ph) PERM(99, d0, c0_, ph) \
                 PERM(100, b1, a1, pl) PERM(101, d1, c1_, pl) PERM(102, b1, a1, ph) PERM(103, d1, c1_, ph) \
                 PERM(104, b2, a2, pl) PERM(105, d2, c2_, pl) PERM(106, b2, a2, ph) PERM(107, d2, c2_, ph) \
                 PERM(108, b3, a3, pl) PERM(109, d3, c3_, pl) PERM(110, b3, a3, ph) PERM(111, d3, c3_, ph) \
                 MF(0, w0, w1, 96, 97) MF(1, w0, w1, 98, 99) MF(2, w0, w1, 100, 101) MF(3, w0, w1, 102, 103) \
                 MF(4, w0, w1, 104, 105) MF(5, w0, w1, 106, 107) MF(6, w0, w1, 108, 109) MF(7, w0, w1, 110, 111)
#define CONS_A   CONS(64, 65, 66, 67, 68, 69, 70, 71, 72, 73, 74, 75, 76, 77, 78, 79, 116, 117)
#define CONS_B   CONS(80, 81, 82, 83, 84, 85, 86, 87, 88, 89, 90, 91, 92, 93, 94, 95, 118, 119)
#define LDP(d0a, d0b, d1a, d1b, d2a, d2b) LD(d0a, d0b, 120) "s_cmp_lt_u32 %[rem], 2\n\ts_cbranch_scc1 1f\n\t" LD(d1a, d1b, 121) \
                 "s_cmp_lt_u32 %[rem], 3\n\ts_cbranch_scc1 1f\n\t" LD(d2a, d2b, 122) "1:\n\t"
#define LDPA     LDP(64, 67, 68, 71, 72, 75)
#define LDPB     LDP(80, 83, 84, 87, 88, 91)
#define STG
#define STG_OP
#define STG_CLOB
        int left = nb;
        int xb = extra_bar;
        asm volatile(
            "s_cmp_lt_i32 %[left], 1\n\t"
            "s_cbranch_scc1 L_empty%=\n\t"
            "s_cmp_gt_i32 %[left], 2\n\t"
            "s_cbranch_scc1 L_go%=\n\t"
            "v_mov_b32 v64, 0\n\tv_mov_b32 v65, 0\n\tv_mov_b32 v66, 0\n\tv_mov_b32 v67, 0\n\tv_mov_b32 v68, 0\n\tv_mov_b32 v69, 0\n\tv_mov_b32 v70, 0\n\tv_mov_b32 v71, 0\n\t"
            "v_mov_b32 v72, 0\n\tv_mov_b32 v73, 0\n\tv_mov_b32 v74, 0\n\tv_mov_b32 v75, 0\n\tv_mov_b32 v76, 0\n\tv_mov_b32 v77, 0\n\tv_mov_b32 v78, 0\n\tv_mov_b32 v79, 0\n\t"
            "v_mov_b32 v80, 0\n\tv_mov_b32 v81, 0\n\tv_mov_b32 v82, 0\n\tv_mov_b32 v83, 0\n\tv_mov_b32 v84, 0\n\tv_mov_b32 v85, 0\n\tv_mov_b32 v86, 0\n\tv_mov_b32 v87, 0\n\t"
            "v_mov_b32 v88, 0\n\tv_mov_b32 v89, 0\n\tv_mov_b32 v90, 0\n\tv_mov_b32 v91, 0\n\tv_mov_b32 v92, 0\n\tv_mov_b32 v93, 0\n\tv_mov_b32 v94, 0\n\tv_mov_b32 v95, 0\n\t"
            "L_go%=:\n\t"
            RD_ENT ADDRS
            "s_sub_i32 %[left], %[left], 1\n\t"
            "s_cmp_lg_u32 %[left], 0\n\t"
            "s_cbranch_scc1 L_p0f%=\n\t"
            "s_cmp_eq_u32 %[rem], 0\n\t"
            "s_cbranch_scc1 L_p0f%=\n\t"
            LDPA
            "s_branch L_p0d%=\n\t"
            "L_p0f%=:\n\t"
            LD4A
            "L_p0d%=:\n\t"
            RD_VALS WEIGHTS(116, 117)
            "L_loop%=:\n\t"
            "s_cmp_lt_i32 %[left], 1\n\t"
            "s_cbranch_scc1 L_finA%=\n\t"
            RD_ENT ADDRS
            "s_sub_i32 %[left], %[left], 1\n\t"
            "s_cmp_lg_u32 %[left], 0\n\t"
            "s_cbranch_scc1 L_bf%=\n\t"
            "s_cmp_eq_u32 %[rem], 0\n\t"
            "s_cbranch_scc1 L_bf%=\n\t"
            LDPB
            RD_VALS
            "s_waitcnt vmcnt(0)\n\t"
            "s_branch L_bd%=\n\t"
            "L_bf%=:\n\t"
            LD4B
            RD_VALS
            "s_waitcnt vmcnt(4)\n\t"
            "L_bd%=:\n\t"
            CONS_A WEIGHTS(118, 119)
            "s_barrier\n\t" STG
            "s_cmp_lt_i32 %[left], 1\n\t"
            "s_cbranch_scc1 L_finB%=\n\t"
            RD_ENT ADDRS
            "s_sub_i32 %[left], %[left], 1\n\t"
            "s_cmp_lg_u32 %[left], 0\n\t"
            "s_cbranch_scc1 L_af%=\n\t"
            "s_cmp_eq_u32 %[rem], 0\n\t"
            "s_cbranch_scc1 L_af%=\n\t"
            LDPA
            RD_VALS
            "s_waitcnt vmcnt(0)\n\t"
            "s_branch L_ad%=\n\t"
            "L_af%=:\n\t"
            LD4A
            RD_VALS
            "s_waitcnt vmcnt(4)\n\t"
            "L_ad%=:\n\t"
            CONS_B WEIGHTS(116, 117)
            "s_barrier\n\t" STG
            "s_branch L_loop%=\n\t"
            "L_finA%=:\n\t"
            "s_cmp_lt_i32 %[xb], 1\n\t"
            "s_cbranch_scc1 L_finA2%=\n\t"
            "s_barrier\n\t"
            "s_sub_i32 %[xb], %[xb], 1\n\t"
            "s_branch L_finA%=\n\t"
            "L_finA2%=:\n\t"
            "s_waitcnt vmcnt(0)\n\t"
            CONS_A
            "s_branch L_done%=\n\t"
            "L_finB%=:\n\t"
            "s_cmp_lt_i32 %[xb], 1\n\t"
            "s_cbranch_scc1 L_finB2%=\n\t"
            "s_barrier\n\t"
            "s_sub_i32 %[xb], %[xb], 1\n\t"
            "s_branch L_finB%=\n\t"
            "L_finB2%=:\n\t"
            "s_waitcnt vmcnt(0)\n\t"
            CONS_B
            "s_branch L_done%=\n\t"
            "L_empty%=:\n\t"
            "s_cmp_lt_i32 %[xb], 2\n\t"
            "s_cbranch_scc1 L_done%=\n\t"
            "s_barrier\n\t"
            "s_sub_i32 %[xb], %[xb], 1\n\t"
            "s_branch L_empty%=\n\t"
            "L_done%=:\n\t"
            "s_nop 7\n\ts_nop 7"
            : [c0] "+v"(acc[0]), [c1] "+v"(acc[1]), [c2] "+v"(acc[2]), [c3] "+v"(acc[3]),
              [c4] "+v"(acc[4]), [c5] "+v"(acc[5]), [c6] "+v"(acc[6]), [c7] "+v"(acc[7]),
              [la] "+v"(laddr), [left] "+s"(left), [xb] "+s"(xb) STG_OP
            : [B] "s"(Bm), [msk] "s"(0x7fff80u), [s16] "v"(sub16), [vr] "v"(vrow), [qi] "v"(qi), [rem] "s"(rem),
              [pl] "s"(0x05040100u), [ph] "s"(0x07060302u)
            : "memory", "scc" STG_CLOB,
              "v64", "v65", "v66", "v67", "v68", "v69", "v70", "v71", "v72", "v73", "v74", "v75", "v76", "v77", "v78", "v79",
              "v80", "v81", "v82", "v83", "v84", "v85", "v86", "v87", "v88", "v89", "v90", "v91", "v92", "v93", "v94", "v95",
              "v96", "v97", "v98", "v99", "v100", "v101", "v102", "v103", "v104", "v105", "v106", "v107", "v108", "v109", "v110", "v111",
              "v112", "v113", "v114", "v115", "v116", "v117", "v118", "v119", "v120", "v121", "v122", "v123", "v124", "v125", "v126", "v127");
    }
    STAMP(7);

#pragma unroll
    for (int s = 0; s < 2; ++s) {
        u32x4 o;
#pragma unroll
        for (int m = 0; m < 4; ++m) {
            const bf16x2 p = __builtin_convertvector(f32x2{acc[2 * m][s], acc[2 * m + 1][s]}, bf16x2);
            o[m] = __builtin_bit_cast(unsigned, p);
        }
        const unsigned r = s ? r1 : r0;
        *(u32x4*)(out + (size_t)(rowBase + r) * 64 + sub * 8) = o;
    }
    STAMP(8);
    asm volatile("s_waitcnt vmcnt(0)" ::: "memory");
    STAMP(9);
    STAMP_FLUSH;
}

extern "C" void kernel_launch(void* const* d_in, const int* in_sizes, int n_in,
                              void* d_out, int out_size, void* d_ws, size_t ws_size,
                              hipStream_t stream) {
    const uint16_t* vals = (const uint16_t*)d_in[0];
    const int* col = (const int*)d_in[1];
    const uint16_t* B = (const uint16_t*)d_in[2];
    uint16_t* out = (uint16_t*)d_out;
    const int M = out_size / 64;
    spmm_ell_kernel<<<M / ROWS, NTHR, 0, stream>>>(vals, col, B, out STAMP_PASS);
}
```

```cpp
#include <hip/hip_runtime.h>
#include <hip/hip_bf16.h>
#include <stdint.h>

typedef __attribute__((ext_vector_type(4))) unsigned u32x4;

__device__ __forceinline__ float bf_lo(unsigned u) { return __uint_as_float(u << 16); }
__device__ __forceinline__ float bf_hi(unsigned u) { return __uint_as_float(u & 0xffff0000u); }

__global__ __launch_bounds__(256) void spmm_naive(const uint16_t* __restrict__ vals, const int* __restrict__ col,
                                                   const uint16_t* __restrict__ B, uint16_t* __restrict__ out,
                                                   int M, int K) {
    const unsigned long long t_start = __builtin_amdgcn_s_memrealtime();
    const int tid = blockIdx.x * 256 + threadIdx.x;
    const int row = tid >> 3;
    const int sub = tid & 7;
    if (row >= M) return;
    const uint16_t* vr = vals + (size_t)row * K;
    const int* cr = col + (size_t)row * K;
    float acc[8];
#pragma unroll
    for (int i = 0; i < 8; ++i) acc[i] = 0.f;
    for (int j0 = 0; j0 < K; j0 += 8) {
        int j = j0 + sub;
        unsigned v = 0; int c = 0;
        if (j < K) { v = vr[j]; c = cr[j]; }
#pragma unroll
        for (int t = 0; t < 8; ++t) {
            unsigned vt = __shfl(v, t, 8);
            int ct = __shfl(c, t, 8);
            if ((vt & 0x7fff) != 0) {
                u32x4 b = *(const u32x4*)(B + (size_t)ct * 64 + sub * 8);
                float f = __uint_as_float(vt << 16);
                acc[0] += f * bf_lo(b[0]); acc[1] += f * bf_hi(b[0]);
                acc[2] += f * bf_lo(b[1]); acc[3] += f * bf_hi(b[1]);
                acc[4] += f * bf_lo(b[2]); acc[5] += f * bf_hi(b[2]);
                acc[6] += f * bf_lo(b[3]); acc[7] += f * bf_hi(b[3]);
            }
        }
    }
    u32x4 o;
#pragma unroll
    for (int i = 0; i < 4; ++i) {
        __hip_bfloat16 lo = __float2bfloat16(acc[2 * i]);
        __hip_bfloat16 hi = __float2bfloat16(acc[2 * i + 1]);
        o[i] = (unsigned)__bfloat16_as_ushort(lo) | ((unsigned)__bfloat16_as_ushort(hi) << 16);
    }
    *(u32x4*)(out + (size_t)row * 64 + sub * 8) = o;
    while (__builtin_amdgcn_s_memrealtime() - t_start < 10000ull) __builtin_amdgcn_s_sleep(1);
}

extern "C" void kernel_launch(void* const* d_in, const int* in_sizes, int n_in,
                              void* d_out, int out_size, void* d_ws, size_t ws_size,
                              hipStream_t stream) {
    const uint16_t* vals = (const uint16_t*)d_in[0];
    const int* col = (const int*)d_in[1];
    const uint16_t* B = (const uint16_t*)d_in[2];
    uint16_t* out = (uint16_t*)d_out;
    int M = out_size / 64;
    int K = in_sizes[0] / M;
    spmm_naive<<<(M * 8 + 255) / 256, 256, 0, stream>>>(vals, col, B, out, M, K);
}
```

```cpp
#include <hip/hip_runtime.h>
#include <stdint.h>

typedef __attribute__((ext_vector_type(4))) unsigned u32x4;
typedef __attribute__((ext_vector_type(2))) unsigned u32x2;
typedef __attribute__((ext_vector_type(4))) float f32x4;
typedef __attribute__((ext_vector_type(2))) float f32x2;
typedef __attribute__((ext_vector_type(2))) __bf16 bf16x2;
typedef unsigned long long u64;
typedef __attribute__((ext_vector_type(2))) u64 u64x2;

constexpr int KS = 65;
constexpr int ROWS = 256;
constexpr int NTHR = 1024;
constexpr int LCAP = 132;
constexpr int PF = 4;

constexpr int OFF_VALS = 0;
constexpr int OFF_IDX = ROWS * KS * 2;
constexpr int OFF_MASK = 128 * LCAP * 8;
constexpr int OFF_HIST = OFF_MASK + ROWS * 8;
constexpr int OFF_RMAP = OFF_HIST + 512;
constexpr int OFF_SYNC = OFF_RMAP + 512;
constexpr int OFF_DUMP = OFF_SYNC + 16;
constexpr int LDS_BYTES = OFF_DUMP + 256;

__device__ __forceinline__ void dma16_nt(const void* gbase, unsigned goff, unsigned lds_dst) {
    unsigned keep;
    asm volatile("s_mov_b32 %0, m0\n\ts_mov_b32 m0, %3\n\ts_nop 0\n\tglobal_load_lds_dwordx4 %1, %2 nt\n\ts_mov_b32 m0, %0"
                 : "=&s"(keep) : "v"(goff), "s"(gbase), "s"(lds_dst) : "memory");
}
__device__ __forceinline__ void dma4(const void* gbase, unsigned goff, unsigned lds_dst) {
    unsigned keep;
    asm volatile("s_mov_b32 %0, m0\n\ts_mov_b32 m0, %3\n\ts_nop 0\n\tglobal_load_lds_dword %1, %2\n\ts_mov_b32 m0, %0"
                 : "=&s"(keep) : "v"(goff), "s"(gbase), "s"(lds_dst) : "memory");
}
__device__ __forceinline__ void lds_signal(unsigned addr) {
    asm volatile("s_waitcnt lgkmcnt(0)\n\ts_mov_b64 exec, 1\n\tds_add_u32 %0, %1\n\ts_mov_b64 exec, -1" :: "v"(addr), "v"(1u) : "memory");
}
__device__ __forceinline__ void lds_wait(unsigned addr, unsigned n) {
    unsigned v, sv;
    asm volatile("1:\n\tds_read_b32 %0, %2\n\ts_waitcnt lgkmcnt(0)\n\tv_readfirstlane_b32 %1, %0\n\ts_cmp_ge_u32 %1, %3\n\ts_cbranch_scc1 2f\n\ts_sleep 2\n\ts_branch 1b\n2:"
                 : "=&v"(v), "=&s"(sv) : "v"(addr), "s"(n) : "memory", "scc");
}
template <int CTRL, int ROWM> __device__ __forceinline__ unsigned dpp(unsigned x) {
    return __builtin_amdgcn_update_dpp(0u, x, CTRL, ROWM, 0xf, true);
}

__global__ __launch_bounds__(NTHR) void spmm_ell_kernel(const uint16_t* __restrict__ vals, const int* __restrict__ col,
                                                        const uint16_t* __restrict__ Bm, uint16_t* __restrict__ out) {
    __shared__ __attribute__((aligned(16))) char smem[LDS_BYTES];
    const int tid = threadIdx.x;
    const int lane = tid & 63;
    const int w = __builtin_amdgcn_readfirstlane(tid >> 6);
    const int rowBase = blockIdx.x * ROWS;
    const unsigned sbase = (unsigned)(size_t)smem;

    u64x2 pp;
    asm volatile("spmm_ell_code_start:\n\ts_load_dwordx4 %0, %1, 0x0\n\ts_waitcnt lgkmcnt(0)" : "=s"(pp) : "s"(__builtin_amdgcn_kernarg_segment_ptr()) : "memory");
    const char* gv = (const char*)pp[0] + (size_t)(rowBase + w * 32) * (KS * 2);
    const unsigned vdst = sbase + OFF_VALS + w * (32 * KS * 2);
    if (w < 8) {
        dma16_nt(gv, lane * 16, vdst);
        dma16_nt(gv, (64 + lane) * 16, vdst + 1024);
    } else if (w == 15) {
        *(u32x2*)(smem + OFF_HIST + lane * 8) = u32x2{0, 0};
        if (lane < 4) *(unsigned*)(smem + OFF_SYNC + lane * 4) = 0;
        asm volatile("s_waitcnt lgkmcnt(0)" ::: "memory");
    }
    asm volatile("s_barrier" ::: "memory");
    u64 unused = 0;
    if (w < 8) {
        dma16_nt(gv, (128 + lane) * 16, vdst + 2048);
        dma16_nt(gv, (192 + lane) * 16, vdst + 3072);
        if (lane < 4) dma16_nt(gv, (256 + lane) * 16, vdst + 4096);
        unsigned pclo, pchi;
        asm volatile("s_getpc_b64 s[40:41]\n"
                     "spmm_ell_code_here:\n\t"
                     "s_sub_u32 %0, s40, (spmm_ell_code_here - spmm_ell_code_start)\n\t"
                     "s_subb_u32 %1, s41, 0"
                     : "=s"(pclo), "=s"(pchi) :: "s40", "s41", "scc");
        const u64 pc = ((((u64)pchi << 32) | pclo) & ~127ull) + (unsigned)w * (9 * 128);
        asm volatile("s_load_dwordx2 %0, %1, 0x3c\n\ts_load_dwordx2 %0, %1, 0xbc\n\ts_load_dwordx2 %0, %1, 0x13c\n\ts_load_dwordx2 %0, %1, 0x1bc\n\t"
                     "s_load_dwordx2 %0, %1, 0x23c\n\ts_load_dwordx2 %0, %1, 0x2bc\n\ts_load_dwordx2 %0, %1, 0x33c\n\ts_load_dwordx2 %0, %1, 0x3bc\n\t"
                     "s_load_dwordx2 %0, %1, 0x43c"
                     : "+s"(unused) : "s"(pc));
    }
    if (w >= 8) {
        const char* gi = (const char*)pp[1] + (size_t)rowBase * (KS * 4);
        const int v = w - 8;
#pragma unroll
        for (int k = 0; k < 8; ++k) dma16_nt(gi, ((k * 8 + v) * 64 + lane) * 16, sbase + OFF_IDX + (k * 8 + v) * 1024);
        if (v == 0) dma16_nt(gi, (4096 + lane) * 16, sbase + OFF_IDX + 4096 * 16);
        lds_signal(sbase + OFF_SYNC + 12);
        lds_wait(sbase + OFF_SYNC + 12, 8);
        if (v < PF) {
            dma4(Bm, (unsigned)(((blockIdx.x >> 3) * 4 + v) * 64 + lane) * 128u, sbase + OFF_DUMP);
            const int i = v * 64 + lane;
            if (i < 7 * 32) dma4(Bm, (((i >> 5) + 1) * 8192u + blockIdx.x * 32u + (i & 31)) * 128u, sbase + OFF_DUMP);
        }
    } else {
        asm volatile("s_waitcnt vmcnt(0) lgkmcnt(0)" : "+s"(unused) :: "memory");

        const int r = w * 32 + (lane >> 1);
        unsigned mh = 0;
        asm volatile(
            "s_waitcnt lgkmcnt(0)\n\t"
            "ds_read_u16 v95, %[va] offset:62\n\t"
            "ds_read_u16 v94, %[va] offset:60\n\t"
            "ds_read_u16 v93, %[va] offset:58\n\t"
            "ds_read_u16 v92, %[va] offset:56\n\t"
            "ds_read_u16 v91, %[va] offset:54\n\t"
            "ds_read_u16 v90, %[va] offset:52\n\t"
            "ds_read_u16 v89, %[va] offset:50\n\t"
            "ds_read_u16 v88, %[va] offset:48\n\t"
            "ds_read_u16 v87, %[va] offset:46\n\t"
            "ds_read_u16 v86, %[va] offset:44\n\t"
            "ds_read_u16 v85, %[va] offset:42\n\t"
            "ds_read_u16 v84, %[va] offset:40\n\t"
            "ds_read_u16 v83, %[va] offset:38\n\t"
            "ds_read_u16 v82, %[va] offset:36\n\t"
            "ds_read_u16 v81, %[va] offset:34\n\t"
            "ds_read_u16 v80, %[va] offset:32\n\t"
            "ds_read_u16 v79, %[va] offset:30\n\t"
            "ds_read_u16 v78, %[va] offset:28\n\t"
            "ds_read_u16 v77, %[va] offset:26\n\t"
            "ds_read_u16 v76, %[va] offset:24\n\t"
            "ds_read_u16 v75, %[va] offset:22\n\t"
            "ds_read_u16 v74, %[va] offset:20\n\t"
            "ds_read_u16 v73, %[va] offset:18\n\t"
            "ds_read_u16 v72, %[va] offset:16\n\t"
            "ds_read_u16 v71, %[va] offset:14\n\t"
            "ds_read_u16 v70, %[va] offset:12\n\t"
            "ds_read_u16 v69, %[va] offset:10\n\t"
            "ds_read_u16 v68, %[va] offset:8\n\t"
            "ds_read_u16 v67, %[va] offset:6\n\t"
            "ds_read_u16 v66, %[va] offset:4\n\t"
            "ds_read_u16 v65, %[va] offset:2\n\t"
            "ds_read_u16 v64, %[va] offset:0\n\t"
            "s_waitcnt lgkmcnt(15)\n\t"
            "v_cmp_class_f16_e64 s[40:41], v95, %[cls]\n\t"
            "v_cmp_class_f16_e64 s[42:43], v94, %[cls]\n\t"
            "v_cmp_class_f16_e64 s[44:45], v93, %[cls]\n\t"
            "v_addc_co_u32_e64 %[m], vcc, %[m], %[m], s[40:41]\n\t"
            "v_addc_co_u32_e64 %[m], vcc, %[m], %[m], s[42:43]\n\t"
            "v_addc_co_u32_e64 %[m], vcc, %[m], %[m], s[44:45]\n\t"
            "v_cmp_class_f16_e64 s[40:41], v92, %[cls]\n\t"
            "v_cmp_class_f16_e64 s[42:43], v91, %[cls]\n\t"
            "v_cmp_class_f16_e64 s[44:45], v90, %[cls]\n\t"
            "v_addc_co_u32_e64 %[m], vcc, %[m], %[m], s[40:41]\n\t"
            "v_addc_co_u32_e64 %[m], vcc, %[m], %[m], s[42:43]\n\t"
            "v_addc_co_u32_e64 %[m], vcc, %[m], %[m], s[44:45]\n\t"
            "v_cmp_class_f16_e64 s[40:41], v89, %[cls]\n\t"
            "v_cmp_class_f16_e64 s[42:43], v88, %[cls]\n\t"
            "v_cmp_class_f16_e64 s[44:45], v87, %[cls]\n\t"
            "v_addc_co_u32_e64 %[m], vcc, %[m], %[m], s[40:41]\n\t"
            "v_addc_co_u32_e64 %[m], vcc, %[m], %[m], s[42:43]\n\t"
            "v_addc_co_u32_e64 %[m], vcc, %[m], %[m], s[44:45]\n\t"
            "v_cmp_class_f16_e64 s[40:41], v86, %[cls]\n\t"
            "v_cmp_class_f16_e64 s[42:43], v85, %[cls]\n\t"
            "v_cmp_class_f16_e64 s[44:45], v84, %[cls]\n\t"
            "v_addc_co_u32_e64 %[m], vcc, %[m], %[m], s[40:41]\n\t"
            "v_addc_co_u32_e64 %[m], vcc, %[m], %[m], s[42:43]\n\t"
            "v_addc_co_u32_e64 %[m], vcc, %[m], %[m], s[44:45]\n\t"
            "v_cmp_class_f16_e64 s[40:41], v83, %[cls]\n\t"
            "v_cmp_class_f16_e64 s[42:43], v82, %[cls]\n\t"
            "v_cmp_class_f16_e64 s[44:45], v81, %[cls]\n\t"
            "v_addc_co_u32_e64 %[m], vcc, %[m], %[m], s[40:41]\n\t"
            "v_addc_co_u32_e64 %[m], vcc, %[m], %[m], s[42:43]\n\t"
            "v_addc_co_u32_e64 %[m], vcc, %[m], %[m], s[44:45]\n\t"
            "s_waitcnt lgkmcnt(7)\n\t"
            "v_cmp_class_f16_e64 s[40:41], v80, %[cls]\n\t"
            "v_cmp_class_f16_e64 s[42:43], v79, %[cls]\n\t"
            "v_cmp_class_f16_e64 s[44:45], v78, %[cls]\n\t"
            "v_addc_co_u32_e64 %[m], vcc, %[m], %[m], s[40:41]\n\t"
            "v_addc_co_u32_e64 %[m], vcc, %[m], %[m], s[42:43]\n\t"
            "v_addc_co_u32_e64 %[m], vcc, %[m], %[m], s[44:45]\n\t"
            "v_cmp_class_f16_e64 s[40:41], v77, %[cls]\n\t"
            "v_cmp_class_f16_e64 s[42:43], v76, %[cls]\n\t"
            "v_cmp_class_f16_e64 s[44:45], v75, %[cls]\n\t"
            "v_addc_co_u32_e64 %[m], vcc, %[m], %[m], s[40:41]\n\t"
            "v_addc_co_u32_e64 %[m], vcc, %[m], %[m], s[42:43]\n\t"
            "v_addc_co_u32_e64 %[m], vcc, %[m], %[m], s[44:45]\n\t"
            "v_cmp_class_f16_e64 s[40:41], v74, %[cls]\n\t"
            "v_cmp_class_f16_e64 s[42:43], v73, %[cls]\n\t"
            "v_cmp_class_f16_e64 s[44:45], v72, %[cls]\n\t"
            "v_addc_co_u32_e64 %[m], vcc, %[m], %[m], s[40:41]\n\t"
            "v_addc_co_u32_e64 %[m], vcc, %[m], %[m], s[42:43]\n\t"
            "v_addc_co_u32_e64 %[m], vcc, %[m], %[m], s[44:45]\n\t"
            "s_waitcnt lgkmcnt(0)\n\t"
            "v_cmp_class_f16_e64 s[40:41], v71, %[cls]\n\t"
            "v_cmp_class_f16_e64 s[42:43], v70, %[cls]\n\t"
            "v_cmp_class_f16_e64 s[44:45], v69, %[cls]\n\t"
            "v_addc_co_u32_e64 %[m], vcc, %[m], %[m], s[40:41]\n\t"
            "v_addc_co_u32_e64 %[m], vcc, %[m], %[m], s[42:43]\n\t"
            "v_addc_co_u32_e64 %[m], vcc, %[m], %[m], s[44:45]\n\t"
            "v_cmp_class_f16_e64 s[40:41], v68, %[cls]\n\t"
            "v_cmp_class_f16_e64 s[42:43], v67, %[cls]\n\t"
            "v_cmp_class_f16_e64 s[44:45], v66, %[cls]\n\t"
            "v_addc_co_u32_e64 %[m], vcc, %[m], %[m], s[40:41]\n\t"
            "v_addc_co_u32_e64 %[m], vcc, %[m], %[m], s[42:43]\n\t"
            "v_addc_co_u32_e64 %[m], vcc, %[m], %[m], s[44:45]\n\t"
            "v_cmp_class_f16_e64 s[40:41], v65, %[cls]\n\t"
            "v_cmp_class_f16_e64 s[42:43], v64, %[cls]\n\t"
            "s_nop 0\n\t"
            "v_addc_co_u32_e64 %[m], vcc, %[m], %[m], s[40:41]\n\t"
            "v_addc_co_u32_e64 %[m], vcc, %[m], %[m], s[42:43]\n\t"
            : [m] "+v"(mh) : [va] "v"(sbase + OFF_VALS + (r * KS + (lane & 1) * 32) * 2), [cls] "s"(0x39fu)
            : "vcc", "memory", "s40", "s41", "s42", "s43", "s44", "s45",
              "v64", "v65", "v66", "v67", "v68", "v69", "v70", "v71", "v72", "v73", "v74", "v75", "v76", "v77", "v78", "v79", "v80", "v81", "v82", "v83", "v84", "v85", "v86", "v87", "v88", "v89", "v90", "v91", "v92", "v93", "v94", "v95");
        const unsigned nz64 = (((const uint16_t*)(smem + OFF_VALS))[r * KS + 64] & 0x7fffu) != 0 ? 1u : 0u;
        *(unsigned*)(smem + OFF_MASK + r * 8 + (lane & 1) * 4) = mh;
        unsigned key = __builtin_popcount(mh);
        key += dpp<0xb1, 0xf>(key) + nz64;
        key = key > 63 ? 63 : key;
        const bool valid = (lane & 1) == 0;
        u64 eq = 0x5555555555555555ull;
#pragma unroll
        for (int b = 0; b < 6; ++b) {
            const int bit = -(int)((key >> b) & 1);
            const u64 bal = __ballot(bit != 0);
            eq &= ~(bal ^ (((u64)(unsigned)bit << 32) | (unsigned)bit));
        }
        const unsigned rank = __builtin_amdgcn_mbcnt_hi((unsigned)(eq >> 32), __builtin_amdgcn_mbcnt_lo((unsigned)eq, 0));
        if (valid && rank == 0) *(uint8_t*)(smem + OFF_HIST + key * 8 + w) = (uint8_t)__builtin_popcountll(eq);
        lds_signal(sbase + OFF_SYNC + 4);
        lds_wait(sbase + OFF_SYNC + 4, 8);
        const u32x2 h = *(const u32x2*)(smem + OFF_HIST + lane * 8);
        const u32x2 hk = *(const u32x2*)(smem + OFF_HIST + key * 8);
        const unsigned tot = __builtin_amdgcn_sad_u8(h[1], 0, __builtin_amdgcn_sad_u8(h[0], 0, 0));
        unsigned inc = tot;
        inc += dpp<0x111, 0xf>(inc);
        inc += dpp<0x112, 0xf>(inc);
        inc += dpp<0x114, 0xf>(inc);
        inc += dpp<0x118, 0xf>(inc);
        inc += dpp<0x142, 0xa>(inc);
        inc += dpp<0x143, 0xc>(inc);
        const unsigned base = __builtin_amdgcn_ds_bpermute(key * 4, inc - tot);
        const unsigned e0 = w >= 4 ? 0xffffffffu : ((1u << (w * 8)) - 1u);
        const unsigned e1 = w <= 4 ? 0u : ((1u << ((w - 4) * 8)) - 1u);
        const unsigned early = __builtin_amdgcn_sad_u8(hk[1] & e1, 0, __builtin_amdgcn_sad_u8(hk[0] & e0, 0, 0));
        const unsigned pos = base + early + rank;
        const unsigned slot = pos < 128 ? pos * 2 : (255 - pos) * 2 + 1;
        if (valid) *(uint16_t*)(smem + OFF_RMAP + slot * 2) = (uint16_t)(r | (nz64 << 8));
        lds_signal(sbase + OFF_SYNC + 8);
    }
    lds_wait(sbase + OFF_SYNC + 8, 8);

    const unsigned sub = lane & 7;
    const int q = tid >> 3;
    const unsigned lbase = sbase + q * (LCAP * 8);
    const int young = w >= 8 && w < 8 + PF ? 2 : 0;

    unsigned rm, len, maxlen;
    asm volatile(
            "ds_read_b32 %[rm], %[ra]\n\t"
            "s_waitcnt lgkmcnt(0)\n\t"
            "v_and_b32 v124, 0xff, %[rm]\n\t"
            "v_bfe_u32 v125, %[rm], 16, 8\n\t"
            "v_lshl_add_u32 v126, v124, 3, %[mb]\n\t"
            "v_lshl_add_u32 v127, v125, 3, %[mb]\n\t"
            "ds_read_b64 v[100:101], v126\n\t"
            "ds_read_b64 v[102:103], v127\n\t"
            "v_bfe_u32 v104, %[rm], 8, 1\n\t"
            "v_bfe_u32 v105, %[rm], 24, 1\n\t"
            "v_mad_u32_u24 v112, v124, %[c130], %[cv]\n\t"
            "v_mad_u32_u24 v113, v125, %[c130], %[cv]\n\t"
            "v_mad_u32_u24 v114, v124, %[c260], %[ci]\n\t"
            "v_mad_u32_u24 v115, v125, %[c260], %[ci]\n\t"
            "v_and_b32 v116, v104, %[s0]\n\t"
            "v_and_b32 v117, v105, %[s0]\n\t"
            "v_mul_u32_u24 v106, 9, v104\n\t"
            "v_mul_u32_u24 v107, 9, v105\n\t"
            "s_waitcnt lgkmcnt(0)\n\t"
            "v_bcnt_u32_b32 %[len], v100, v104\n\t"
            "v_or_b32 v108, 1, v100\n\t"
            "v_or_b32 v109, 1, v102\n\t"
            "v_bcnt_u32_b32 %[len], v101, %[len]\n\t"
            "v_ffbh_u32 v108, v108\n\t"
            "v_ffbh_u32 v109, v109\n\t"
            "v_bcnt_u32_b32 %[len], v102, %[len]\n\t"
            "v_ffbh_u32 v110, v101\n\t"
            "v_ffbh_u32 v111, v103\n\t"
            "v_bcnt_u32_b32 %[len], v103, %[len]\n\t"
            "v_add_u32 v108, 32, v108\n\t"
            "v_add_u32 v109, 32, v109\n\t"
            "v_add_u32 %[len], %[len], v105\n\t"
            "v_min_u32 v108, v108, v110\n\t"
            "v_min_u32 v109, v109, v111\n\t"
            "v_sub_u32 v108, 71, v108\n\t"
            "v_sub_u32 v109, 71, v109\n\t"
            "v_lshrrev_b32 v108, 3, v108\n\t"
            "v_lshrrev_b32 v109, 3, v109\n\t"
            "v_max_u32 v108, v108, v106\n\t"
            "v_max_u32 v109, v109, v107\n\t"
            "v_lshrrev_b64 v[100:101], %[sub], v[100:101]\n\t"
            "v_lshrrev_b64 v[102:103], %[sub], v[102:103]\n\t"
            "v_max_u32_dpp v110, %[len], %[len] row_shr:8 row_mask:0xf bank_mask:0xf bound_ctrl:1\n\t"
            "v_max_u32_dpp v108, v108, v108 row_shr:8 row_mask:0xf bank_mask:0xf bound_ctrl:1\n\t"
            "v_max_u32_dpp v109, v109, v109 row_shr:8 row_mask:0xf bank_mask:0xf bound_ctrl:1\n\t"
            "v_max_u32_dpp v110, v110, v110 row_bcast:15 row_mask:0xa bank_mask:0xf\n\t"
            "v_max_u32_dpp v108, v108, v108 row_bcast:15 row_mask:0xa bank_mask:0xf\n\t"
            "v_max_u32_dpp v109, v109, v109 row_bcast:15 row_mask:0xa bank_mask:0xf\n\t"
            "v_max_u32_dpp v110, v110, v110 row_bcast:31 row_mask:0xc bank_mask:0xf\n\t"
            "v_max_u32_dpp v108, v108, v108 row_bcast:31 row_mask:0xc bank_mask:0xf\n\t"
            "v_max_u32_dpp v109, v109, v109 row_bcast:31 row_mask:0xc bank_mask:0xf\n\t"
            "v_and_b32 v120, 0x01010101, v100\n\t"
            "v_and_b32 v121, 0x01010101, v101\n\t"
            "v_readlane_b32 %[ml], v110, 63\n\t"
            "v_readlane_b32 s78, v108, 63\n\t"
            "v_readlane_b32 s79, v109, 63\n\t"
            "v_and_b32 v122, 0x01010101, v102\n\t"
            "v_and_b32 v123, 0x01010101, v103\n\t"
            "v_mov_b32 v110, %[ml]\n\t"
            "s_mov_b64 exec, 1\n\t"
            "ds_max_u32 %[nba], v110\n\t"
            "s_mov_b64 exec, -1\n\t"
            "s_sub_u32 s78, 9, s78\n\t"
            "s_sub_u32 s79, 9, s79\n\t"
            "s_getpc_b64 s[76:77]\n\t"
            "Lb%=:\n"
            "s_mul_i32 s80, s78, (Lvr0_7%= - Lvr0_8%=)\n\t"
            "s_add_u32 s80, s80, (Lvr0_8%= - Lb%=)\n\t"
            "s_add_u32 s80, s76, s80\n\t"
            "s_addc_u32 s81, s77, 0\n\t"
            "s_mul_i32 s82, s79, (Lvr1_7%= - Lvr1_8%=)\n\t"
            "s_add_u32 s82, s82, (Lvr1_8%= - Lb%=)\n\t"
            "s_add_u32 s82, s76, s82\n\t"
            "s_addc_u32 s83, s77, 0\n\t"
            "s_setpc_b64 s[80:81]\n\t"
            "Lvr0_8%=:\n"
            "ds_read_u16 v81, v112 offset:128\n\t"
            "Lvr0_7%=:\n"
            "ds_read_u16 v79, v112 offset:112\n\t"
            "Lvr0_6%=:\n"
            "ds_read_u16 v77, v112 offset:96\n\t"
            "Lvr0_5%=:\n"
            "ds_read_u16 v75, v112 offset:80\n\t"
            "Lvr0_4%=:\n"
            "ds_read_u16 v73, v112 offset:64\n\t"
            "Lvr0_3%=:\n"
            "ds_read_u16 v71, v112 offset:48\n\t"
            "Lvr0_2%=:\n"
            "ds_read_u16 v69, v112 offset:32\n\t"
            "Lvr0_1%=:\n"
            "ds_read_u16 v67, v112 offset:16\n\t"
            "Lvr0_0%=:\n"
            "ds_read_u16 v65, v112 offset:0\n\t"
            "s_setpc_b64 s[82:83]\n\t"
            "Lvr1_8%=:\n"
            "ds_read_u16 v99, v113 offset:128\n\t"
            "Lvr1_7%=:\n"
            "ds_read_u16 v97, v113 offset:112\n\t"
            "Lvr1_6%=:\n"
            "ds_read_u16 v95, v113 offset:96\n\t"
            "Lvr1_5%=:\n"
            "ds_read_u16 v93, v113 offset:80\n\t"
            "Lvr1_4%=:\n"
            "ds_read_u16 v91, v113 offset:64\n\t"
            "Lvr1_3%=:\n"
            "ds_read_u16 v89, v113 offset:48\n\t"
            "Lvr1_2%=:\n"
            "ds_read_u16 v87, v113 offset:32\n\t"
            "Lvr1_1%=:\n"
            "ds_read_u16 v85, v113 offset:16\n\t"
            "Lvr1_0%=:\n"
            "ds_read_u16 v83, v113 offset:0\n\t"
            "v_cmp_ne_u32_sdwa s[40:41], v120, 0 src0_sel:BYTE_0 src1_sel:DWORD\n\t"
            "v_cmp_ne_u32_sdwa s[42:43], v120, 0 src0_sel:BYTE_1 src1_sel:DWORD\n\t"
            "v_cmp_ne_u32_sdwa s[44:45], v120, 0 src0_sel:BYTE_2 src1_sel:DWORD\n\t"
            "v_cmp_ne_u32_sdwa s[46:47], v120, 0 src0_sel:BYTE_3 src1_sel:DWORD\n\t"
            "v_cmp_ne_u32_sdwa s[48:49], v121, 0 src0_sel:BYTE_0 src1_sel:DWORD\n\t"
            "v_cmp_ne_u32_sdwa s[50:51], v121, 0 src0_sel:BYTE_1 src1_sel:DWORD\n\t"
            "v_cmp_ne_u32_sdwa s[52:53], v121, 0 src0_sel:BYTE_2 src1_sel:DWORD\n\t"
            "v_cmp_ne_u32_sdwa s[54:55], v121, 0 src0_sel:BYTE_3 src1_sel:DWORD\n\t"
            "v_cmp_ne_u32_e64 s[56:57], 0, v116\n\t"
            "v_cmp_ne_u32_sdwa s[58:59], v122, 0 src0_sel:BYTE_0 src1_sel:DWORD\n\t"
            "v_cmp_ne_u32_sdwa s[60:61], v122, 0 src0_sel:BYTE_1 src1_sel:DWORD\n\t"
            "v_cmp_ne_u32_sdwa s[62:63], v122, 0 src0_sel:BYTE_2 src1_sel:DWORD\n\t"
            "v_cmp_ne_u32_sdwa s[64:65], v122, 0 src0_sel:BYTE_3 src1_sel:DWORD\n\t"
            "v_cmp_ne_u32_sdwa s[66:67], v123, 0 src0_sel:BYTE_0 src1_sel:DWORD\n\t"
            "v_cmp_ne_u32_sdwa s[68:69], v123, 0 src0_sel:BYTE_1 src1_sel:DWORD\n\t"
            "v_cmp_ne_u32_sdwa s[70:71], v123, 0 src0_sel:BYTE_2 src1_sel:DWORD\n\t"
            "v_cmp_ne_u32_sdwa s[72:73], v123, 0 src0_sel:BYTE_3 src1_sel:DWORD\n\t"
            "v_cmp_ne_u32_e64 s[74:75], 0, v117\n\t"
            "s_mul_i32 s80, s78, (Lir0_7%= - Lir0_8%=)\n\t"
            "s_add_u32 s80, s80, (Lir0_8%= - Lb%=)\n\t"
            "s_add_u32 s80, s76, s80\n\t"
            "s_addc_u32 s81, s77, 0\n\t"
            "s_mul_i32 s82, s79, (Lir1_7%= - Lir1_8%=)\n\t"
            "s_add_u32 s82, s82, (Lir1_8%= - Lb%=)\n\t"
            "s_add_u32 s82, s76, s82\n\t"
            "s_addc_u32 s83, s77, 0\n\t"
            "s_mul_i32 s84, s78, (Lpa0_7%= - Lpa0_8%=)\n\t"
            "s_add_u32 s84, s84, (Lpa0_8%= - Lb%=)\n\t"
            "s_add_u32 s84, s76, s84\n\t"
            "s_addc_u32 s85, s77, 0\n\t"
            "s_mul_i32 s86, s79, (Lpa1_7%= - Lpa1_8%=)\n\t"
            "s_add_u32 s86, s86, (Lpa1_8%= - Lb%=)\n\t"
            "s_add_u32 s86, s76, s86\n\t"
            "s_addc_u32 s87, s77, 0\n\t"
            "s_mul_i32 s88, s78, (Lpb0_7%= - Lpb0_8%=)\n\t"
            "s_add_u32 s88, s88, (Lpb0_8%= - Lb%=)\n\t"
            "s_add_u32 s88, s76, s88\n\t"
            "s_addc_u32 s89, s77, 0\n\t"
            "s_mul_i32 s90, s79, (Lpb1_7%= - Lpb1_8%=)\n\t"
            "s_add_u32 s90, s90, (Lpb1_8%= - Lb%=)\n\t"
            "s_add_u32 s90, s76, s90\n\t"
            "s_addc_u32 s91, s77, 0\n\t"
            "v_sub_u32 v43, 0, %[sub]\n\t"
            "v_sub_u32 v44, 1, %[sub]\n\t"
            "v_sub_u32 v45, 3, %[sub]\n\t"
            "v_mov_b32 v118, 0\n\t"
            "v_mov_b32 v119, 0\n\t"
            "v_ashrrev_i32 v43, 31, v43\n\t"
            "v_ashrrev_i32 v44, 31, v44\n\t"
            "v_ashrrev_i32 v45, 31, v45\n\t"
            "s_waitcnt lgkmcnt(0)\n\t"
            "v_lshlrev_b32 v83, 16, v83\n\t"
            "v_lshlrev_b32 v85, 16, v85\n\t"
            "v_lshlrev_b32 v87, 16, v87\n\t"
            "v_lshlrev_b32 v89, 16, v89\n\t"
            "v_lshlrev_b32 v91, 16, v91\n\t"
            "v_lshlrev_b32 v93, 16, v93\n\t"
            "v_lshlrev_b32 v95, 16, v95\n\t"
            "v_lshlrev_b32 v97, 16, v97\n\t"
            "v_lshlrev_b32 v99, 16, v99\n\t"
            "s_cmp_eq_u32 %[young], 0\n\t"
            "s_cbranch_scc1 Lw0%=\n\t"
            "s_cmp_eq_u32 %[young], 1\n\t"
            "s_cbranch_scc1 Lw1%=\n\t"
            "s_cmp_eq_u32 %[young], 2\n\t"
            "s_cbranch_scc1 Lw2%=\n\t"
            "s_waitcnt vmcnt(3)\n\t"
            "s_branch Lwd%=\n\t"
            "Lw2%=:\n"
            "s_waitcnt vmcnt(2)\n\t"
            "s_branch Lwd%=\n\t"
            "Lw1%=:\n"
            "s_waitcnt vmcnt(1)\n\t"
            "s_branch Lwd%=\n\t"
            "Lw0%=:\n"
            "s_waitcnt vmcnt(0)\n\t"
            "Lwd%=:\n"
            "s_barrier\n\t"
            "s_setpc_b64 s[80:81]\n\t"
            "Lir0_8%=:\n"
            "ds_read_b32 v80, v114 offset:256\n\t"
            "Lir0_7%=:\n"
            "ds_read_b32 v78, v114 offset:224\n\t"
            "Lir0_6%=:\n"
            "ds_read_b32 v76, v114 offset:192\n\t"
            "Lir0_5%=:\n"
            "ds_read_b32 v74, v114 offset:160\n\t"
            "Lir0_4%=:\n"
            "ds_read_b32 v72, v114 offset:128\n\t"
            "Lir0_3%=:\n"
            "ds_read_b32 v70, v114 offset:96\n\t"
            "Lir0_2%=:\n"
            "ds_read_b32 v68, v114 offset:64\n\t"
            "Lir0_1%=:\n"
            "ds_read_b32 v66, v114 offset:32\n\t"
            "Lir0_0%=:\n"
            "ds_read_b32 v64, v114 offset:0\n\t"
            "s_setpc_b64 s[82:83]\n\t"
            "Lir1_8%=:\n"
            "ds_read_b32 v98, v115 offset:256\n\t"
            "Lir1_7%=:\n"
            "ds_read_b32 v96, v115 offset:224\n\t"
            "Lir1_6%=:\n"
            "ds_read_b32 v94, v115 offset:192\n\t"
            "Lir1_5%=:\n"
            "ds_read_b32 v92, v115 offset:160\n\t"
            "Lir1_4%=:\n"
            "ds_read_b32 v90, v115 offset:128\n\t"
            "Lir1_3%=:\n"
            "ds_read_b32 v88, v115 offset:96\n\t"
            "Lir1_2%=:\n"
            "ds_read_b32 v86, v115 offset:64\n\t"
            "Lir1_1%=:\n"
            "ds_read_b32 v84, v115 offset:32\n\t"
            "Lir1_0%=:\n"
            "ds_read_b32 v82, v115 offset:0\n\t"
            "s_waitcnt lgkmcnt(0)\n\t"
            "s_barrier\n\t"
            "s_setpc_b64 s[84:85]\n\t"
            "Lpa0_8%=:\n"
            "s_mov_b64 exec, s[56:57]\n\t"
            "v_lshrrev_b32 v108, 13, v80\n\t"
            "v_lshlrev_b32 v120, 2, v108\n\t"
            "v_lshlrev_b32 v80, 7, v80\n\t"
            "v_bfe_u32 v54, v118, v120, 4\n\t"
            "v_lshl_add_u32 v118, 1, v120, v118\n\t"
            "Lpa0_7%=:\n"
            "s_mov_b64 exec, s[54:55]\n\t"
            "v_lshrrev_b32 v107, 13, v78\n\t"
            "v_lshlrev_b32 v120, 2, v107\n\t"
            "v_lshlrev_b32 v78, 7, v78\n\t"
            "v_bfe_u32 v53, v118, v120, 4\n\t"
            "v_lshl_add_u32 v118, 1, v120, v118\n\t"
            "Lpa0_6%=:\n"
            "s_mov_b64 exec, s[52:53]\n\t"
            "v_lshrrev_b32 v106, 13, v76\n\t"
            "v_lshlrev_b32 v120, 2, v106\n\t"
            "v_lshlrev_b32 v76, 7, v76\n\t"
            "v_bfe_u32 v52, v118, v120, 4\n\t"
            "v_lshl_add_u32 v118, 1, v120, v118\n\t"
            "Lpa0_5%=:\n"
            "s_mov_b64 exec, s[50:51]\n\t"
            "v_lshrrev_b32 v105, 13, v74\n\t"
            "v_lshlrev_b32 v120, 2, v105\n\t"
            "v_lshlrev_b32 v74, 7, v74\n\t"
            "v_bfe_u32 v51, v118, v120, 4\n\t"
            "v_lshl_add_u32 v118, 1, v120, v118\n\t"
            "Lpa0_4%=:\n"
            "s_mov_b64 exec, s[48:49]\n\t"
            "v_lshrrev_b32 v104, 13, v72\n\t"
            "v_lshlrev_b32 v120, 2, v104\n\t"
            "v_lshlrev_b32 v72, 7, v72\n\t"
            "v_bfe_u32 v50, v118, v120, 4\n\t"
            "v_lshl_add_u32 v118, 1, v120, v118\n\t"
            "Lpa0_3%=:\n"
            "s_mov_b64 exec, s[46:47]\n\t"
            "v_lshrrev_b32 v103, 13, v70\n\t"
            "v_lshlrev_b32 v120, 2, v103\n\t"
            "v_lshlrev_b32 v70, 7, v70\n\t"
            "v_bfe_u32 v49, v118, v120, 4\n\t"
            "v_lshl_add_u32 v118, 1, v120, v118\n\t"
            "Lpa0_2%=:\n"
            "s_mov_b64 exec, s[44:45]\n\t"
            "v_lshrrev_b32 v102, 13, v68\n\t"
            "v_lshlrev_b32 v120, 2, v102\n\t"
            "v_lshlrev_b32 v68, 7, v68\n\t"
            "v_bfe_u32 v48, v118, v120, 4\n\t"
            "v_lshl_add_u32 v118, 1, v120, v118\n\t"
            "Lpa0_1%=:\n"
            "s_mov_b64 exec, s[42:43]\n\t"
            "v_lshrrev_b32 v101, 13, v66\n\t"
            "v_lshlrev_b32 v120, 2, v101\n\t"
            "v_lshlrev_b32 v66, 7, v66\n\t"
            "v_bfe_u32 v47, v118, v120, 4\n\t"
            "v_lshl_add_u32 v118, 1, v120, v118\n\t"
            "Lpa0_0%=:\n"
            "s_mov_b64 exec, s[40:41]\n\t"
            "v_lshrrev_b32 v100, 13, v64\n\t"
            "v_lshlrev_b32 v120, 2, v100\n\t"
            "v_lshlrev_b32 v64, 7, v64\n\t"
            "v_bfe_u32 v46, v118, v120, 4\n\t"
            "v_lshl_add_u32 v118, 1, v120, v118\n\t"
            "s_setpc_b64 s[86:87]\n\t"
            "Lpa1_8%=:\n"
            "s_mov_b64 exec, s[74:75]\n\t"
            "v_lshrrev_b32 v117, 13, v98\n\t"
            "v_lshlrev_b32 v120, 2, v117\n\t"
            "v_lshlrev_b32 v98, 7, v98\n\t"
            "v_bfe_u32 v63, v119, v120, 4\n\t"
            "v_lshl_add_u32 v119, 1, v120, v119\n\t"
            "Lpa1_7%=:\n"
            "s_mov_b64 exec, s[72:73]\n\t"
            "v_lshrrev_b32 v116, 13, v96\n\t"
            "v_lshlrev_b32 v120, 2, v116\n\t"
            "v_lshlrev_b32 v96, 7, v96\n\t"
            "v_bfe_u32 v62, v119, v120, 4\n\t"
            "v_lshl_add_u32 v119, 1, v120, v119\n\t"
            "Lpa1_6%=:\n"
            "s_mov_b64 exec, s[70:71]\n\t"
            "v_lshrrev_b32 v115, 13, v94\n\t"
            "v_lshlrev_b32 v120, 2, v115\n\t"
            "v_lshlrev_b32 v94, 7, v94\n\t"
            "v_bfe_u32 v61, v119, v120, 4\n\t"
            "v_lshl_add_u32 v119, 1, v120, v119\n\t"
            "Lpa1_5%=:\n"
            "s_mov_b64 exec, s[68:69]\n\t"
            "v_lshrrev_b32 v114, 13, v92\n\t"
            "v_lshlrev_b32 v120, 2, v114\n\t"
            "v_lshlrev_b32 v92, 7, v92\n\t"
            "v_bfe_u32 v60, v119, v120, 4\n\t"
            "v_lshl_add_u32 v119, 1, v120, v119\n\t"
            "Lpa1_4%=:\n"
            "s_mov_b64 exec, s[66:67]\n\t"
            "v_lshrrev_b32 v113, 13, v90\n\t"
            "v_lshlrev_b32 v120, 2, v113\n\t"
            "v_lshlrev_b32 v90, 7, v90\n\t"
            "v_bfe_u32 v59, v119, v120, 4\n\t"
            "v_lshl_add_u32 v119, 1, v120, v119\n\t"
            "Lpa1_3%=:\n"
            "s_mov_b64 exec, s[64:65]\n\t"
            "v_lshrrev_b32 v112, 13, v88\n\t"
            "v_lshlrev_b32 v120, 2, v112\n\t"
            "v_lshlrev_b32 v88, 7, v88\n\t"
            "v_bfe_u32 v58, v119, v120, 4\n\t"
            "v_lshl_add_u32 v119, 1, v120, v119\n\t"
            "Lpa1_2%=:\n"
            "s_mov_b64 exec, s[62:63]\n\t"
            "v_lshrrev_b32 v111, 13, v86\n\t"
            "v_lshlrev_b32 v120, 2, v111\n\t"
            "v_lshlrev_b32 v86, 7, v86\n\t"
            "v_bfe_u32 v57, v119, v120, 4\n\t"
            "v_lshl_add_u32 v119, 1, v120, v119\n\t"
            "Lpa1_1%=:\n"
            "s_mov_b64 exec, s[60:61]\n\t"
            "v_lshrrev_b32 v110, 13, v84\n\t"
            "v_lshlrev_b32 v120, 2, v110\n\t"
            "v_lshlrev_b32 v84, 7, v84\n\t"
            "v_bfe_u32 v56, v119, v120, 4\n\t"
            "v_lshl_add_u32 v119, 1, v120, v119\n\t"
            "Lpa1_0%=:\n"
            "s_mov_b64 exec, s[58:59]\n\t"
            "v_lshrrev_b32 v109, 13, v82\n\t"
            "v_lshlrev_b32 v120, 2, v109\n\t"
            "v_lshlrev_b32 v82, 7, v82\n\t"
            "v_bfe_u32 v55, v119, v120, 4\n\t"
            "v_lshl_add_u32 v119, 1, v120, v119\n\t"
            "s_mov_b64 exec, -1\n\t"
            "s_mov_b32 s78, 0x0f0f0f0f\n\t"
            "v_and_b32 v120, s78, v118\n\t"
            "v_lshrrev_b32 v121, 4, v118\n\t"
            "v_and_b32 v122, s78, v119\n\t"
            "v_lshrrev_b32 v123, 4, v119\n\t"
            "v_and_b32 v121, s78, v121\n\t"
            "v_and_b32 v123, s78, v123\n\t"
            "v_add_u32 v124, v120, v122\n\t"
            "v_add_u32 v125, v121, v123\n\t"
            "s_nop 1\n\t"
            "v_and_b32_dpp v118, v124, v43 row_shr:1 row_mask:0xf bank_mask:0xf bound_ctrl:1\n\t"
            "v_and_b32_dpp v119, v125, v43 row_shr:1 row_mask:0xf bank_mask:0xf bound_ctrl:1\n\t"
            "v_add_u32 v126, v118, v124\n\t"
            "v_add_u32 v127, v119, v125\n\t"
            "s_nop 0\n\t"
            "v_and_b32_dpp v118, v126, v44 row_shr:2 row_mask:0xf bank_mask:0xf bound_ctrl:1\n\t"
            "v_and_b32_dpp v119, v127, v44 row_shr:2 row_mask:0xf bank_mask:0xf bound_ctrl:1\n\t"
            "v_add_u32 v126, v118, v126\n\t"
            "v_add_u32 v127, v119, v127\n\t"
            "s_nop 0\n\t"
            "v_and_b32_dpp v118, v126, v45 row_shr:4 row_mask:0xf bank_mask:0xf bound_ctrl:1\n\t"
            "v_and_b32_dpp v119, v127, v45 row_shr:4 row_mask:0xf bank_mask:0xf bound_ctrl:1\n\t"
            "v_add_u32 v126, v118, v126\n\t"
            "v_add_u32 v127, v119, v127\n\t"
            "s_nop 0\n\t"
            "v_mov_b32_dpp v118, v126 row_newbcast:7 row_mask:0xf bank_mask:0x3\n\t"
            "v_mov_b32_dpp v119, v127 row_newbcast:7 row_mask:0xf bank_mask:0x3\n\t"
            "v_mov_b32_dpp v118, v126 row_newbcast:15 row_mask:0xf bank_mask:0xc\n\t"
            "v_mov_b32_dpp v119, v127 row_newbcast:15 row_mask:0xf bank_mask:0xc\n\t"
            "s_mov_b32 s78, 0x03030303\n\t"
            "v_lshl_add_u32 v118, v118, 8, v118\n\t"
            "v_lshl_add_u32 v119, v119, 8, v119\n\t"
            "v_lshl_add_u32 v118, v118, 16, v118\n\t"
            "v_lshl_add_u32 v119, v119, 16, v119\n\t"
            "v_sub_u32 v126, v126, v124\n\t"
            "v_sub_u32 v127, v127, v125\n\t"
            "v_perm_b32 v124, v118, v118, s78\n\t"
            "v_lshl_add_u32 v126, v118, 8, v126\n\t"
            "v_add_u32 v119, v119, v124\n\t"
            "s_mov_b32 s78, 0x05010400\n\t"
            "s_mov_b32 s79, 0x07030602\n\t"
            "v_alignbit_b32 v119, v119, v118, 24\n\t"
            "v_add_u32 v127, v127, v119\n\t"
            "v_perm_b32 v118, v127, v126, s78\n\t"
            "v_perm_b32 v119, v127, v126, s79\n\t"
            "v_add_u32 v126, v126, v120\n\t"
            "v_add_u32 v127, v127, v121\n\t"
            "v_perm_b32 v124, v127, v126, s78\n\t"
            "v_perm_b32 v125, v127, v126, s79\n\t"
            "s_setpc_b64 s[88:89]\n\t"
            "Lpb0_8%=:\n"
            "s_mov_b64 exec, s[56:57]\n\t"
            "v_perm_b32 v120, v119, v118, v108\n\t"
            "v_add_u32_sdwa v120, v120, v54 dst_sel:DWORD dst_unused:UNUSED_PAD src0_sel:BYTE_0 src1_sel:DWORD\n\t"
            "v_lshl_add_u32 v120, v120, 3, %[lb]\n\t"
            "ds_write_b64 v120, v[80:81]\n\t"
            "Lpb0_7%=:\n"
            "s_mov_b64 exec, s[54:55]\n\t"
            "v_perm_b32 v120, v119, v118, v107\n\t"
            "v_add_u32_sdwa v120, v120, v53 dst_sel:DWORD dst_unused:UNUSED_PAD src0_sel:BYTE_0 src1_sel:DWORD\n\t"
            "v_lshl_add_u32 v120, v120, 3, %[lb]\n\t"
            "ds_write_b64 v120, v[78:79]\n\t"
            "Lpb0_6%=:\n"
            "s_mov_b64 exec, s[52:53]\n\t"
            "v_perm_b32 v120, v119, v118, v106\n\t"
            "v_add_u32_sdwa v120, v120, v52 dst_sel:DWORD dst_unused:UNUSED_PAD src0_sel:BYTE_0 src1_sel:DWORD\n\t"
            "v_lshl_add_u32 v120, v120, 3, %[lb]\n\t"
            "ds_write_b64 v120, v[76:77]\n\t"
            "Lpb0_5%=:\n"
            "s_mov_b64 exec, s[50:51]\n\t"
            "v_perm_b32 v120, v119, v118, v105\n\t"
            "v_add_u32_sdwa v120, v120, v51 dst_sel:DWORD dst_unused:UNUSED_PAD src0_sel:BYTE_0 src1_sel:DWORD\n\t"
            "v_lshl_add_u32 v120, v120, 3, %[lb]\n\t"
            "ds_write_b64 v120, v[74:75]\n\t"
            "Lpb0_4%=:\n"
            "s_mov_b64 exec, s[48:49]\n\t"
            "v_perm_b32 v120, v119, v118, v104\n\t"
            "v_add_u32_sdwa v120, v120, v50 dst_sel:DWORD dst_unused:UNUSED_PAD src0_sel:BYTE_0 src1_sel:DWORD\n\t"
            "v_lshl_add_u32 v120, v120, 3, %[lb]\n\t"
            "ds_write_b64 v120, v[72:73]\n\t"
            "Lpb0_3%=:\n"
            "s_mov_b64 exec, s[46:47]\n\t"
            "v_perm_b32 v120, v119, v118, v103\n\t"
            "v_add_u32_sdwa v120, v120, v49 dst_sel:DWORD dst_unused:UNUSED_PAD src0_sel:BYTE_0 src1_sel:DWORD\n\t"
            "v_lshl_add_u32 v120, v120, 3, %[lb]\n\t"
            "ds_write_b64 v120, v[70:71]\n\t"
            "Lpb0_2%=:\n"
            "s_mov_b64 exec, s[44:45]\n\t"
            "v_perm_b32 v120, v119, v118, v102\n\t"
            "v_add_u32_sdwa v120, v120, v48 dst_sel:DWORD dst_unused:UNUSED_PAD src0_sel:BYTE_0 src1_sel:DWORD\n\t"
            "v_lshl_add_u32 v120, v120, 3, %[lb]\n\t"
            "ds_write_b64 v120, v[68:69]\n\t"
            "Lpb0_1%=:\n"
            "s_mov_b64 exec, s[42:43]\n\t"
            "v_perm_b32 v120, v119, v118, v101\n\t"
            "v_add_u32_sdwa v120, v120, v47 dst_sel:DWORD dst_unused:UNUSED_PAD src0_sel:BYTE_0 src1_sel:DWORD\n\t"
            "v_lshl_add_u32 v120, v120, 3, %[lb]\n\t"
            "ds_write_b64 v120, v[66:67]\n\t"
            "Lpb0_0%=:\n"
            "s_mov_b64 exec, s[40:41]\n\t"
            "v_perm_b32 v120, v119, v118, v100\n\t"
            "v_add_u32_sdwa v120, v120, v46 dst_sel:DWORD dst_unused:UNUSED_PAD src0_sel:BYTE_0 src1_sel:DWORD\n\t"
            "v_lshl_add_u32 v120, v120, 3, %[lb]\n\t"
            "ds_write_b64 v120, v[64:65]\n\t"
            "s_setpc_b64 s[90:91]\n\t"
            "Lpb1_8%=:\n"
            "s_mov_b64 exec, s[74:75]\n\t"
            "v_perm_b32 v120, v125, v124, v117\n\t"
            "v_add_u32_sdwa v120, v120, v63 dst_sel:DWORD dst_unused:UNUSED_PAD src0_sel:BYTE_0 src1_sel:DWORD\n\t"
            "v_lshl_add_u32 v120, v120, 3, %[lb]\n\t"
            "ds_write_b64 v120, v[98:99]\n\t"
            "Lpb1_7%=:\n"
            "s_mov_b64 exec, s[72:73]\n\t"
            "v_perm_b32 v120, v125, v124, v116\n\t"
            "v_add_u32_sdwa v120, v120, v62 dst_sel:DWORD dst_unused:UNUSED_PAD src0_sel:BYTE_0 src1_sel:DWORD\n\t"
            "v_lshl_add_u32 v120, v120, 3, %[lb]\n\t"
            "ds_write_b64 v120, v[96:97]\n\t"
            "Lpb1_6%=:\n"
            "s_mov_b64 exec, s[70:71]\n\t"
            "v_perm_b32 v120, v125, v124, v115\n\t"
            "v_add_u32_sdwa v120, v120, v61 dst_sel:DWORD dst_unused:UNUSED_PAD src0_sel:BYTE_0 src1_sel:DWORD\n\t"
            "v_lshl_add_u32 v120, v120, 3, %[lb]\n\t"
            "ds_write_b64 v120, v[94:95]\n\t"
            "Lpb1_5%=:\n"
            "s_mov_b64 exec, s[68:69]\n\t"
            "v_perm_b32 v120, v125, v124, v114\n\t"
            "v_add_u32_sdwa v120, v120, v60 dst_sel:DWORD dst_unused:UNUSED_PAD src0_sel:BYTE_0 src1_sel:DWORD\n\t"
            "v_lshl_add_u32 v120, v120, 3, %[lb]\n\t"
            "ds_write_b64 v120, v[92:93]\n\t"
            "Lpb1_4%=:\n"
            "s_mov_b64 exec, s[66:67]\n\t"
            "v_perm_b32 v120, v125, v124, v113\n\t"
            "v_add_u32_sdwa v120, v120, v59 dst_sel:DWORD dst_unused:UNUSED_PAD src0_sel:BYTE_0 src1_sel:DWORD\n\t"
            "v_lshl_add_u32 v120, v120, 3, %[lb]\n\t"
            "ds_write_b64 v120, v[90:91]\n\t"
            "Lpb1_3%=:\n"
            "s_mov_b64 exec, s[64:65]\n\t"
            "v_perm_b32 v120, v125, v124, v112\n\t"
            "v_add_u32_sdwa v120, v120, v58 dst_sel:DWORD dst_unused:UNUSED_PAD src0_sel:BYTE_0 src1_sel:DWORD\n\t"
            "v_lshl_add_u32 v120, v120, 3, %[lb]\n\t"
            "ds_write_b64 v120, v[88:89]\n\t"
            "Lpb1_2%=:\n"
            "s_mov_b64 exec, s[62:63]\n\t"
            "v_perm_b32 v120, v125, v124, v111\n\t"
            "v_add_u32_sdwa v120, v120, v57 dst_sel:DWORD dst_unused:UNUSED_PAD src0_sel:BYTE_0 src1_sel:DWORD\n\t"
            "v_lshl_add_u32 v120, v120, 3, %[lb]\n\t"
            "ds_write_b64 v120, v[86:87]\n\t"
            "Lpb1_1%=:\n"
            "s_mov_b64 exec, s[60:61]\n\t"
            "v_perm_b32 v120, v125, v124, v110\n\t"
            "v_add_u32_sdwa v120, v120, v56 dst_sel:DWORD dst_unused:UNUSED_PAD src0_sel:BYTE_0 src1_sel:DWORD\n\t"
            "v_lshl_add_u32 v120, v120, 3, %[lb]\n\t"
            "ds_write_b64 v120, v[84:85]\n\t"
            "Lpb1_0%=:\n"
            "s_mov_b64 exec, s[58:59]\n\t"
            "v_perm_b32 v120, v125, v124, v109\n\t"
            "v_add_u32_sdwa v120, v120, v55 dst_sel:DWORD dst_unused:UNUSED_PAD src0_sel:BYTE_0 src1_sel:DWORD\n\t"
            "v_lshl_add_u32 v120, v120, 3, %[lb]\n\t"
            "ds_write_b64 v120, v[82:83]\n\t"
            "s_mov_b64 exec, -1\n\t"
            : [rm] "=&v"(rm), [len] "=&v"(len), [ml] "=&s"(maxlen)
            : [ra] "v"(sbase + OFF_RMAP + q * 4), [mb] "s"(sbase + OFF_MASK), [c130] "s"(KS * 2), [c260] "s"(KS * 4),
              [cv] "v"(sbase + OFF_VALS + sub * 2), [ci] "v"(sbase + OFF_IDX + sub * 4), [s0] "v"(sub == 0 ? 1u : 0u),
              [nba] "v"(sbase + OFF_SYNC), [lb] "v"(lbase), [sub] "v"(sub), [young] "s"(young)
            : "memory", "scc", "vcc",
              "s40", "s41", "s42", "s43", "s44", "s45", "s46", "s47", "s48", "s49", "s50", "s51", "s52", "s53", "s54", "s55", "s56", "s57", "s58", "s59", "s60", "s61", "s62", "s63", "s64", "s65", "s66", "s67", "s68", "s69", "s70", "s71", "s72", "s73", "s74", "s75", "s76", "s77", "s78", "s79", "s80", "s81", "s82", "s83", "s84", "s85", "s86", "s87", "s88", "s89", "s90", "s91",
              "v43", "v44", "v45", "v46", "v47", "v48", "v49", "v50", "v51", "v52", "v53", "v54", "v55", "v56", "v57", "v58", "v59", "v60", "v61", "v62", "v63", "v64", "v65", "v66", "v67", "v68", "v69", "v70", "v71", "v72", "v73", "v74", "v75", "v76", "v77", "v78", "v79", "v80", "v81", "v82", "v83", "v84", "v85", "v86", "v87", "v88", "v89", "v90", "v91", "v92", "v93", "v94", "v95", "v96", "v97", "v98", "v99", "v100", "v101", "v102", "v103", "v104", "v105", "v106", "v107", "v108", "v109", "v110", "v111", "v112", "v113", "v114", "v115", "v116", "v117", "v118", "v119", "v120", "v121", "v122", "v123", "v124", "v125", "v126", "v127");
    const unsigned r0 = rm & 0xff, r1 = (rm >> 16) & 0xff;
    const int nb = (int)(maxlen + 3) >> 2;
    for (unsigned p = len + sub; p < (unsigned)nb * 4; p += 8) *(u32x2*)(smem + q * (LCAP * 8) + p * 8) = u32x2{0, 0};
    const unsigned longest = *(const unsigned*)(smem + OFF_SYNC);
    asm volatile("s_waitcnt lgkmcnt(0)" ::: "memory");

    f32x4 acc[8];
    {
        unsigned laddr = lbase;
        int n;
        asm volatile(
            "s_cmp_lt_i32 %[nb], 1\n\t"
            "s_cbranch_scc1 Lempty%=\n\t"
            "s_cmp_gt_i32 %[nb], 2\n\t"
            "s_cbranch_scc1 Lgo%=\n\t"
            "v_mov_b32 v64, 0\n\t"
            "v_mov_b32 v65, 0\n\t"
            "v_mov_b32 v66, 0\n\t"
            "v_mov_b32 v67, 0\n\t"
            "v_mov_b32 v68, 0\n\t"
            "v_mov_b32 v69, 0\n\t"
            "v_mov_b32 v70, 0\n\t"
            "v_mov_b32 v71, 0\n\t"
            "v_mov_b32 v72, 0\n\t"
            "v_mov_b32 v73, 0\n\t"
            "v_mov_b32 v74, 0\n\t"
            "v_mov_b32 v75, 0\n\t"
            "v_mov_b32 v76, 0\n\t"
            "v_mov_b32 v77, 0\n\t"
            "v_mov_b32 v78, 0\n\t"
            "v_mov_b32 v79, 0\n\t"
            "v_mov_b32 v80, 0\n\t"
            "v_mov_b32 v81, 0\n\t"
            "v_mov_b32 v82, 0\n\t"
            "v_mov_b32 v83, 0\n\t"
            "v_mov_b32 v84, 0\n\t"
            "v_mov_b32 v85, 0\n\t"
            "v_mov_b32 v86, 0\n\t"
            "v_mov_b32 v87, 0\n\t"
            "v_mov_b32 v88, 0\n\t"
            "v_mov_b32 v89, 0\n\t"
            "v_mov_b32 v90, 0\n\t"
            "v_mov_b32 v91, 0\n\t"
            "v_mov_b32 v92, 0\n\t"
            "v_mov_b32 v93, 0\n\t"
            "v_mov_b32 v94, 0\n\t"
            "v_mov_b32 v95, 0\n\t"
            "Lgo%=:\n"
            "ds_read_b128 v[112:115], %[la]\n\t"
            "ds_read_b128 v[116:119], %[la] offset:16\n\t"
            "v_add_u32 %[la], 32, %[la]\n\t"
            "s_waitcnt lgkmcnt(0)\n\t"
            "v_or_b32 v120, v112, %[s16]\n\t"
            "v_or_b32 v121, v114, %[s16]\n\t"
            "v_or_b32 v122, v116, %[s16]\n\t"
            "v_or_b32 v123, v118, %[s16]\n\t"
            "v_perm_b32 v124, v115, v113, %[ws]\n\t"
            "v_perm_b32 v125, v119, v117, %[ws]\n\t"
            "s_cmp_gt_i32 %[nb], 1\n\t"
            "s_cbranch_scc1 Lm%=\n\t"
            "s_cmp_eq_u32 %[rem], 0\n\t"
            "s_cbranch_scc1 Lsf%=\n\t"
            "global_load_dwordx4 v[64:67], v120, %[B]\n\t"
            "s_cmp_lt_u32 %[rem], 2\n\t"
            "s_cbranch_scc1 Lsp%=\n\t"
            "global_load_dwordx4 v[68:71], v121, %[B]\n\t"
            "s_cmp_lt_u32 %[rem], 3\n\t"
            "s_cbranch_scc1 Lsp%=\n\t"
            "global_load_dwordx4 v[72:75], v122, %[B]\n\t"
            "Lsp%=:\n"
            "s_branch Lse%=\n\t"
            "Lsf%=:\n"
            "global_load_dwordx4 v[64:67], v120, %[B]\n\t"
            "global_load_dwordx4 v[68:71], v121, %[B]\n\t"
            "global_load_dwordx4 v[72:75], v122, %[B]\n\t"
            "global_load_dwordx4 v[76:79], v123, %[B]\n\t"
            "Lse%=:\n"
            "v_mov_b64 v[8:9], 0\n\t"
            "v_mov_b64 v[10:11], 0\n\t"
            "v_mov_b64 v[12:13], 0\n\t"
            "v_mov_b64 v[14:15], 0\n\t"
            "v_mov_b64 v[16:17], 0\n\t"
            "v_mov_b64 v[18:19], 0\n\t"
            "v_mov_b64 v[20:21], 0\n\t"
            "v_mov_b64 v[22:23], 0\n\t"
            "v_mov_b64 v[24:25], 0\n\t"
            "v_mov_b64 v[26:27], 0\n\t"
            "v_mov_b64 v[28:29], 0\n\t"
            "v_mov_b64 v[30:31], 0\n\t"
            "v_mov_b64 v[32:33], 0\n\t"
            "v_mov_b64 v[34:35], 0\n\t"
            "v_mov_b64 v[36:37], 0\n\t"
            "v_mov_b64 v[38:39], 0\n\t"
            "s_barrier\n\t"
            "s_branch LlastA%=\n\t"
            "Lm%=:\n"
            "global_load_dwordx4 v[64:67], v120, %[B]\n\t"
            "global_load_dwordx4 v[68:71], v121, %[B]\n\t"
            "global_load_dwordx4 v[72:75], v122, %[B]\n\t"
            "global_load_dwordx4 v[76:79], v123, %[B]\n\t"
            "ds_read_b128 v[112:115], %[la]\n\t"
            "ds_read_b128 v[116:119], %[la] offset:16\n\t"
            "v_add_u32 %[la], 32, %[la]\n\t"
            "s_waitcnt lgkmcnt(0)\n\t"
            "v_or_b32 v120, v112, %[s16]\n\t"
            "v_or_b32 v121, v114, %[s16]\n\t"
            "v_or_b32 v122, v116, %[s16]\n\t"
            "v_or_b32 v123, v118, %[s16]\n\t"
            "v_perm_b32 v126, v115, v113, %[ws]\n\t"
            "v_perm_b32 v127, v119, v117, %[ws]\n\t"
            "v_mov_b64 v[8:9], 0\n\t"
            "v_mov_b64 v[10:11], 0\n\t"
            "v_mov_b64 v[12:13], 0\n\t"
            "v_mov_b64 v[14:15], 0\n\t"
            "v_mov_b64 v[16:17], 0\n\t"
            "v_mov_b64 v[18:19], 0\n\t"
            "v_mov_b64 v[20:21], 0\n\t"
            "v_mov_b64 v[22:23], 0\n\t"
            "v_mov_b64 v[24:25], 0\n\t"
            "v_mov_b64 v[26:27], 0\n\t"
            "v_mov_b64 v[28:29], 0\n\t"
            "v_mov_b64 v[30:31], 0\n\t"
            "v_mov_b64 v[32:33], 0\n\t"
            "v_mov_b64 v[34:35], 0\n\t"
            "v_mov_b64 v[36:37], 0\n\t"
            "v_mov_b64 v[38:39], 0\n\t"
            "s_sub_u32 %[n], %[nb], 2\n\t"
            "s_cmp_lg_u32 %[grp], 0\n\t"
            "s_cbranch_scc1 LloopB%=\n\t"
            "LloopA%=:\n"
            "s_sub_u32 %[n], %[n], 1\n\t"
            "s_cbranch_scc1 LtailA%=\n\t"
            "global_load_dwordx4 v[80:83], v120, %[B]\n\t"
            "global_load_dwordx4 v[84:87], v121, %[B]\n\t"
            "global_load_dwordx4 v[88:91], v122, %[B]\n\t"
            "global_load_dwordx4 v[92:95], v123, %[B]\n\t"
            "ds_read_b128 v[112:115], %[la]\n\t"
            "ds_read_b128 v[116:119], %[la] offset:16\n\t"
            "v_add_u32 %[la], 32, %[la]\n\t"
            "s_waitcnt vmcnt(4)\n\t"
            "v_perm_b32 v96, v68, v64, %[pl]\n\t"
            "v_perm_b32 v97, v76, v72, %[pl]\n\t"
            "v_perm_b32 v98, v68, v64, %[ph]\n\t"
            "v_perm_b32 v99, v76, v72, %[ph]\n\t"
            "v_perm_b32 v100, v69, v65, %[pl]\n\t"
            "v_perm_b32 v101, v77, v73, %[pl]\n\t"
            "v_perm_b32 v102, v69, v65, %[ph]\n\t"
            "v_perm_b32 v103, v77, v73, %[ph]\n\t"
            "v_perm_b32 v104, v70, v66, %[pl]\n\t"
            "v_perm_b32 v105, v78, v74, %[pl]\n\t"
            "v_perm_b32 v106, v70, v66, %[ph]\n\t"
            "v_perm_b32 v107, v78, v74, %[ph]\n\t"
            "v_perm_b32 v108, v71, v67, %[pl]\n\t"
            "v_perm_b32 v109, v79, v75, %[pl]\n\t"
            "v_perm_b32 v110, v71, v67, %[ph]\n\t"
            "v_perm_b32 v111, v79, v75, %[ph]\n\t"
            "v_mfma_f32_4x4x4_16b_bf16 v[8:11], v[124:125], v[96:97], v[8:11]\n\t"
            "v_mfma_f32_4x4x4_16b_bf16 v[12:15], v[124:125], v[98:99], v[12:15]\n\t"
            "v_mfma_f32_4x4x4_16b_bf16 v[16:19], v[124:125], v[100:101], v[16:19]\n\t"
            "v_mfma_f32_4x4x4_16b_bf16 v[20:23], v[124:125], v[102:103], v[20:23]\n\t"
            "v_mfma_f32_4x4x4_16b_bf16 v[24:27], v[124:125], v[104:105], v[24:27]\n\t"
            "v_mfma_f32_4x4x4_16b_bf16 v[28:31], v[124:125], v[106:107], v[28:31]\n\t"
            "v_mfma_f32_4x4x4_16b_bf16 v[32:35], v[124:125], v[108:109], v[32:35]\n\t"
            "v_mfma_f32_4x4x4_16b_bf16 v[36:39], v[124:125], v[110:111], v[36:39]\n\t"
            "s_waitcnt lgkmcnt(0)\n\t"
            "v_or_b32 v120, v112, %[s16]\n\t"
            "v_or_b32 v121, v114, %[s16]\n\t"
            "v_or_b32 v122, v116, %[s16]\n\t"
            "v_or_b32 v123, v118, %[s16]\n\t"
            "v_perm_b32 v124, v115, v113, %[ws]\n\t"
            "v_perm_b32 v125, v119, v117, %[ws]\n\t"
            "s_barrier\n\t"
            "s_sub_u32 %[n], %[n], 1\n\t"
            "s_cbranch_scc1 LtailB%=\n\t"
            "global_load_dwordx4 v[64:67], v120, %[B]\n\t"
            "global_load_dwordx4 v[68:71], v121, %[B]\n\t"
            "global_load_dwordx4 v[72:75], v122, %[B]\n\t"
            "global_load_dwordx4 v[76:79], v123, %[B]\n\t"
            "ds_read_b128 v[112:115], %[la]\n\t"
            "ds_read_b128 v[116:119], %[la] offset:16\n\t"
            "v_add_u32 %[la], 32, %[la]\n\t"
            "s_waitcnt vmcnt(4)\n\t"
            "v_perm_b32 v96, v84, v80, %[pl]\n\t"
            "v_perm_b32 v97, v92, v88, %[pl]\n\t"
            "v_perm_b32 v98, v84, v80, %[ph]\n\t"
            "v_perm_b32 v99, v92, v88, %[ph]\n\t"
            "v_perm_b32 v100, v85, v81, %[pl]\n\t"
            "v_perm_b32 v101, v93, v89, %[pl]\n\t"
            "v_perm_b32 v102, v85, v81, %[ph]\n\t"
            "v_perm_b32 v103, v93, v89, %[ph]\n\t"
            "v_perm_b32 v104, v86, v82, %[pl]\n\t"
            "v_perm_b32 v105, v94, v90, %[pl]\n\t"
            "v_perm_b32 v106, v86, v82, %[ph]\n\t"
            "v_perm_b32 v107, v94, v90, %[ph]\n\t"
            "v_perm_b32 v108, v87, v83, %[pl]\n\t"
            "v_perm_b32 v109, v95, v91, %[pl]\n\t"
            "v_perm_b32 v110, v87, v83, %[ph]\n\t"
            "v_perm_b32 v111, v95, v91, %[ph]\n\t"
            "v_mfma_f32_4x4x4_16b_bf16 v[8:11], v[126:127], v[96:97], v[8:11]\n\t"
            "v_mfma_f32_4x4x4_16b_bf16 v[12:15], v[126:127], v[98:99], v[12:15]\n\t"
            "v_mfma_f32_4x4x4_16b_bf16 v[16:19], v[126:127], v[100:101], v[16:19]\n\t"
            "v_mfma_f32_4x4x4_16b_bf16 v[20:23], v[126:127], v[102:103], v[20:23]\n\t"
            "v_mfma_f32_4x4x4_16b_bf16 v[24:27], v[126:127], v[104:105], v[24:27]\n\t"
            "v_mfma_f32_4x4x4_16b_bf16 v[28:31], v[126:127], v[106:107], v[28:31]\n\t"
            "v_mfma_f32_4x4x4_16b_bf16 v[32:35], v[126:127], v[108:109], v[32:35]\n\t"
            "v_mfma_f32_4x4x4_16b_bf16 v[36:39], v[126:127], v[110:111], v[36:39]\n\t"
            "s_waitcnt lgkmcnt(0)\n\t"
            "v_or_b32 v120, v112, %[s16]\n\t"
            "v_or_b32 v121, v114, %[s16]\n\t"
            "v_or_b32 v122, v116, %[s16]\n\t"
            "v_or_b32 v123, v118, %[s16]\n\t"
            "v_perm_b32 v126, v115, v113, %[ws]\n\t"
            "v_perm_b32 v127, v119, v117, %[ws]\n\t"
            "s_barrier\n\t"
            "s_branch LloopA%=\n\t"
            "LloopB%=:\n"
            "s_sub_u32 %[n], %[n], 1\n\t"
            "s_cbranch_scc1 LtailA%=\n\t"
            "global_load_dwordx4 v[80:83], v120, %[B]\n\t"
            "global_load_dwordx4 v[84:87], v121, %[B]\n\t"
            "global_load_dwordx4 v[88:91], v122, %[B]\n\t"
            "global_load_dwordx4 v[92:95], v123, %[B]\n\t"
            "s_barrier\n\t"
            "ds_read_b128 v[112:115], %[la]\n\t"
            "ds_read_b128 v[116:119], %[la] offset:16\n\t"
            "v_add_u32 %[la], 32, %[la]\n\t"
            "s_waitcnt vmcnt(4)\n\t"
            "v_perm_b32 v96, v68, v64, %[pl]\n\t"
            "v_perm_b32 v97, v76, v72, %[pl]\n\t"
            "v_perm_b32 v98, v68, v64, %[ph]\n\t"
            "v_perm_b32 v99, v76, v72, %[ph]\n\t"
            "v_perm_b32 v100, v69, v65, %[pl]\n\t"
            "v_perm_b32 v101, v77, v73, %[pl]\n\t"
            "v_perm_b32 v102, v69, v65, %[ph]\n\t"
            "v_perm_b32 v103, v77, v73, %[ph]\n\t"
            "v_perm_b32 v104, v70, v66, %[pl]\n\t"
            "v_perm_b32 v105, v78, v74, %[pl]\n\t"
            "v_perm_b32 v106, v70, v66, %[ph]\n\t"
            "v_perm_b32 v107, v78, v74, %[ph]\n\t"
            "v_perm_b32 v108, v71, v67, %[pl]\n\t"
            "v_perm_b32 v109, v79, v75, %[pl]\n\t"
            "v_perm_b32 v110, v71, v67, %[ph]\n\t"
            "v_perm_b32 v111, v79, v75, %[ph]\n\t"
            "v_mfma_f32_4x4x4_16b_bf16 v[8:11], v[124:125], v[96:97], v[8:11]\n\t"
            "v_mfma_f32_4x4x4_16b_bf16 v[12:15], v[124:125], v[98:99], v[12:15]\n\t"
            "v_mfma_f32_4x4x4_16b_bf16 v[16:19], v[124:125], v[100:101], v[16:19]\n\t"
            "v_mfma_f32_4x4x4_16b_bf16 v[20:23], v[124:125], v[102:103], v[20:23]\n\t"
            "v_mfma_f32_4x4x4_16b_bf16 v[24:27], v[124:125], v[104:105], v[24:27]\n\t"
            "v_mfma_f32_4x4x4_16b_bf16 v[28:31], v[124:125], v[106:107], v[28:31]\n\t"
            "v_mfma_f32_4x4x4_16b_bf16 v[32:35], v[124:125], v[108:109], v[32:35]\n\t"
            "v_mfma_f32_4x4x4_16b_bf16 v[36:39], v[124:125], v[110:111], v[36:39]\n\t"
            "s_waitcnt lgkmcnt(0)\n\t"
            "v_or_b32 v120, v112, %[s16]\n\t"
            "v_or_b32 v121, v114, %[s16]\n\t"
            "v_or_b32 v122, v116, %[s16]\n\t"
            "v_or_b32 v123, v118, %[s16]\n\t"
            "v_perm_b32 v124, v115, v113, %[ws]\n\t"
            "v_perm_b32 v125, v119, v117, %[ws]\n\t"
            "s_sub_u32 %[n], %[n], 1\n\t"
            "s_cbranch_scc1 LtailB%=\n\t"
            "global_load_dwordx4 v[64:67], v120, %[B]\n\t"
            "global_load_dwordx4 v[68:71], v121, %[B]\n\t"
            "global_load_dwordx4 v[72:75], v122, %[B]\n\t"
            "global_load_dwordx4 v[76:79], v123, %[B]\n\t"
            "s_barrier\n\t"
            "ds_read_b128 v[112:115], %[la]\n\t"
            "ds_read_b128 v[116:119], %[la] offset:16\n\t"
            "v_add_u32 %[la], 32, %[la]\n\t"
            "s_waitcnt vmcnt(4)\n\t"
            "v_perm_b32 v96, v84, v80, %[pl]\n\t"
            "v_perm_b32 v97, v92, v88, %[pl]\n\t"
            "v_perm_b32 v98, v84, v80, %[ph]\n\t"
            "v_perm_b32 v99, v92, v88, %[ph]\n\t"
            "v_perm_b32 v100, v85, v81, %[pl]\n\t"
            "v_perm_b32 v101, v93, v89, %[pl]\n\t"
            "v_perm_b32 v102, v85, v81, %[ph]\n\t"
            "v_perm_b32 v103, v93, v89, %[ph]\n\t"
            "v_perm_b32 v104, v86, v82, %[pl]\n\t"
            "v_perm_b32 v105, v94, v90, %[pl]\n\t"
            "v_perm_b32 v106, v86, v82, %[ph]\n\t"
            "v_perm_b32 v107, v94, v90, %[ph]\n\t"
            "v_perm_b32 v108, v87, v83, %[pl]\n\t"
            "v_perm_b32 v109, v95, v91, %[pl]\n\t"
            "v_perm_b32 v110, v87, v83, %[ph]\n\t"
            "v_perm_b32 v111, v95, v91, %[ph]\n\t"
            "v_mfma_f32_4x4x4_16b_bf16 v[8:11], v[126:127], v[96:97], v[8:11]\n\t"
            "v_mfma_f32_4x4x4_16b_bf16 v[12:15], v[126:127], v[98:99], v[12:15]\n\t"
            "v_mfma_f32_4x4x4_16b_bf16 v[16:19], v[126:127], v[100:101], v[16:19]\n\t"
            "v_mfma_f32_4x4x4_16b_bf16 v[20:23], v[126:127], v[102:103], v[20:23]\n\t"
            "v_mfma_f32_4x4x4_16b_bf16 v[24:27], v[126:127], v[104:105], v[24:27]\n\t"
            "v_mfma_f32_4x4x4_16b_bf16 v[28:31], v[126:127], v[106:107], v[28:31]\n\t"
            "v_mfma_f32_4x4x4_16b_bf16 v[32:35], v[126:127], v[108:109], v[32:35]\n\t"
            "v_mfma_f32_4x4x4_16b_bf16 v[36:39], v[126:127], v[110:111], v[36:39]\n\t"
            "s_waitcnt lgkmcnt(0)\n\t"
            "v_or_b32 v120, v112, %[s16]\n\t"
            "v_or_b32 v121, v114, %[s16]\n\t"
            "v_or_b32 v122, v116, %[s16]\n\t"
            "v_or_b32 v123, v118, %[s16]\n\t"
            "v_perm_b32 v126, v115, v113, %[ws]\n\t"
            "v_perm_b32 v127, v119, v117, %[ws]\n\t"
            "s_branch LloopB%=\n\t"
            "LtailA%=:\n"
            "s_cmp_eq_u32 %[rem], 0\n\t"
            "s_cbranch_scc1 LtAf%=\n\t"
            "global_load_dwordx4 v[80:83], v120, %[B]\n\t"
            "s_cmp_lt_u32 %[rem], 2\n\t"
            "s_cbranch_scc1 LtAp%=\n\t"
            "global_load_dwordx4 v[84:87], v121, %[B]\n\t"
            "s_cmp_lt_u32 %[rem], 3\n\t"
            "s_cbranch_scc1 LtAp%=\n\t"
            "global_load_dwordx4 v[88:91], v122, %[B]\n\t"
            "LtAp%=:\n"
            "s_barrier\n\t"
            "s_waitcnt vmcnt(0)\n\t"
            "s_branch LtAe%=\n\t"
            "LtAf%=:\n"
            "global_load_dwordx4 v[80:83], v120, %[B]\n\t"
            "global_load_dwordx4 v[84:87], v121, %[B]\n\t"
            "global_load_dwordx4 v[88:91], v122, %[B]\n\t"
            "global_load_dwordx4 v[92:95], v123, %[B]\n\t"
            "s_barrier\n\t"
            "s_waitcnt vmcnt(4)\n\t"
            "LtAe%=:\n"
            "v_perm_b32 v96, v68, v64, %[pl]\n\t"
            "v_perm_b32 v97, v76, v72, %[pl]\n\t"
            "v_perm_b32 v98, v68, v64, %[ph]\n\t"
            "v_perm_b32 v99, v76, v72, %[ph]\n\t"
            "v_perm_b32 v100, v69, v65, %[pl]\n\t"
            "v_perm_b32 v101, v77, v73, %[pl]\n\t"
            "v_perm_b32 v102, v69, v65, %[ph]\n\t"
            "v_perm_b32 v103, v77, v73, %[ph]\n\t"
            "v_perm_b32 v104, v70, v66, %[pl]\n\t"
            "v_perm_b32 v105, v78, v74, %[pl]\n\t"
            "v_perm_b32 v106, v70, v66, %[ph]\n\t"
            "v_perm_b32 v107, v78, v74, %[ph]\n\t"
            "v_perm_b32 v108, v71, v67, %[pl]\n\t"
            "v_perm_b32 v109, v79, v75, %[pl]\n\t"
            "v_perm_b32 v110, v71, v67, %[ph]\n\t"
            "v_perm_b32 v111, v79, v75, %[ph]\n\t"
            "s_nop 1\n\t"
            "v_mfma_f32_4x4x4_16b_bf16 v[8:11], v[124:125], v[96:97], v[8:11]\n\t"
            "v_mfma_f32_4x4x4_16b_bf16 v[12:15], v[124:125], v[98:99], v[12:15]\n\t"
            "v_mfma_f32_4x4x4_16b_bf16 v[16:19], v[124:125], v[100:101], v[16:19]\n\t"
            "v_mfma_f32_4x4x4_16b_bf16 v[20:23], v[124:125], v[102:103], v[20:23]\n\t"
            "v_mfma_f32_4x4x4_16b_bf16 v[24:27], v[124:125], v[104:105], v[24:27]\n\t"
            "v_mfma_f32_4x4x4_16b_bf16 v[28:31], v[124:125], v[106:107], v[28:31]\n\t"
            "v_mfma_f32_4x4x4_16b_bf16 v[32:35], v[124:125], v[108:109], v[32:35]\n\t"
            "v_mfma_f32_4x4x4_16b_bf16 v[36:39], v[124:125], v[110:111], v[36:39]\n\t"
            "LlastB%=:\n"
            "s_waitcnt vmcnt(0)\n\t"
            "v_perm_b32 v96, v84, v80, %[pl]\n\t"
            "v_perm_b32 v97, v92, v88, %[pl]\n\t"
            "v_perm_b32 v98, v84, v80, %[ph]\n\t"
            "v_perm_b32 v99, v92, v88, %[ph]\n\t"
            "v_perm_b32 v100, v85, v81, %[pl]\n\t"
            "v_perm_b32 v101, v93, v89, %[pl]\n\t"
            "v_perm_b32 v102, v85, v81, %[ph]\n\t"
            "v_perm_b32 v103, v93, v89, %[ph]\n\t"
            "v_perm_b32 v104, v86, v82, %[pl]\n\t"
            "v_perm_b32 v105, v94, v90, %[pl]\n\t"
            "v_perm_b32 v106, v86, v82, %[ph]\n\t"
            "v_perm_b32 v107, v94, v90, %[ph]\n\t"
            "v_perm_b32 v108, v87, v83, %[pl]\n\t"
            "v_perm_b32 v109, v95, v91, %[pl]\n\t"
            "v_perm_b32 v110, v87, v83, %[ph]\n\t"
            "v_perm_b32 v111, v95, v91, %[ph]\n\t"
            "s_nop 1\n\t"
            "v_mfma_f32_4x4x4_16b_bf16 v[8:11], v[126:127], v[96:97], v[8:11]\n\t"
            "v_mfma_f32_4x4x4_16b_bf16 v[12:15], v[126:127], v[98:99], v[12:15]\n\t"
            "v_mfma_f32_4x4x4_16b_bf16 v[16:19], v[126:127], v[100:101], v[16:19]\n\t"
            "v_mfma_f32_4x4x4_16b_bf16 v[20:23], v[126:127], v[102:103], v[20:23]\n\t"
            "v_mfma_f32_4x4x4_16b_bf16 v[24:27], v[126:127], v[104:105], v[24:27]\n\t"
            "v_mfma_f32_4x4x4_16b_bf16 v[28:31], v[126:127], v[106:107], v[28:31]\n\t"
            "v_mfma_f32_4x4x4_16b_bf16 v[32:35], v[126:127], v[108:109], v[32:35]\n\t"
            "v_mfma_f32_4x4x4_16b_bf16 v[36:39], v[126:127], v[110:111], v[36:39]\n\t"
            "s_branch Ldone%=\n\t"
            "LtailB%=:\n"
            "s_cmp_eq_u32 %[rem], 0\n\t"
            "s_cbranch_scc1 LtBf%=\n\t"
            "global_load_dwordx4 v[64:67], v120, %[B]\n\t"
            "s_cmp_lt_u32 %[rem], 2\n\t"
            "s_cbranch_scc1 LtBp%=\n\t"
            "global_load_dwordx4 v[68:71], v121, %[B]\n\t"
            "s_cmp_lt_u32 %[rem], 3\n\t"
            "s_cbranch_scc1 LtBp%=\n\t"
            "global_load_dwordx4 v[72:75], v122, %[B]\n\t"
            "LtBp%=:\n"
            "s_barrier\n\t"
            "s_waitcnt vmcnt(0)\n\t"
            "s_branch LtBe%=\n\t"
            "LtBf%=:\n"
            "global_load_dwordx4 v[64:67], v120, %[B]\n\t"
            "global_load_dwordx4 v[68:71], v121, %[B]\n\t"
            "global_load_dwordx4 v[72:75], v122, %[B]\n\t"
            "global_load_dwordx4 v[76:79], v123, %[B]\n\t"
            "s_barrier\n\t"
            "s_waitcnt vmcnt(4)\n\t"
            "LtBe%=:\n"
            "v_perm_b32 v96, v84, v80, %[pl]\n\t"
            "v_perm_b32 v97, v92, v88, %[pl]\n\t"
            "v_perm_b32 v98, v84, v80, %[ph]\n\t"
            "v_perm_b32 v99, v92, v88, %[ph]\n\t"
            "v_perm_b32 v100, v85, v81, %[pl]\n\t"
            "v_perm_b32 v101, v93, v89, %[pl]\n\t"
            "v_perm_b32 v102, v85, v81, %[ph]\n\t"
            "v_perm_b32 v103, v93, v89, %[ph]\n\t"
            "v_perm_b32 v104, v86, v82, %[pl]\n\t"
            "v_perm_b32 v105, v94, v90, %[pl]\n\t"
            "v_perm_b32 v106, v86, v82, %[ph]\n\t"
            "v_perm_b32 v107, v94, v90, %[ph]\n\t"
            "v_perm_b32 v108, v87, v83, %[pl]\n\t"
            "v_perm_b32 v109, v95, v91, %[pl]\n\t"
            "v_perm_b32 v110, v87, v83, %[ph]\n\t"
            "v_perm_b32 v111, v95, v91, %[ph]\n\t"
            "s_nop 1\n\t"
            "v_mfma_f32_4x4x4_16b_bf16 v[8:11], v[126:127], v[96:97], v[8:11]\n\t"
            "v_mfma_f32_4x4x4_16b_bf16 v[12:15], v[126:127], v[98:99], v[12:15]\n\t"
            "v_mfma_f32_4x4x4_16b_bf16 v[16:19], v[126:127], v[100:101], v[16:19]\n\t"
            "v_mfma_f32_4x4x4_16b_bf16 v[20:23], v[126:127], v[102:103], v[20:23]\n\t"
            "v_mfma_f32_4x4x4_16b_bf16 v[24:27], v[126:127], v[104:105], v[24:27]\n\t"
            "v_mfma_f32_4x4x4_16b_bf16 v[28:31], v[126:127], v[106:107], v[28:31]\n\t"
            "v_mfma_f32_4x4x4_16b_bf16 v[32:35], v[126:127], v[108:109], v[32:35]\n\t"
            "v_mfma_f32_4x4x4_16b_bf16 v[36:39], v[126:127], v[110:111], v[36:39]\n\t"
            "LlastA%=:\n"
            "s_waitcnt vmcnt(0)\n\t"
            "v_perm_b32 v96, v68, v64, %[pl]\n\t"
            "v_perm_b32 v97, v76, v72, %[pl]\n\t"
            "v_perm_b32 v98, v68, v64, %[ph]\n\t"
            "v_perm_b32 v99, v76, v72, %[ph]\n\t"
            "v_perm_b32 v100, v69, v65, %[pl]\n\t"
            "v_perm_b32 v101, v77, v73, %[pl]\n\t"
            "v_perm_b32 v102, v69, v65, %[ph]\n\t"
            "v_perm_b32 v103, v77, v73, %[ph]\n\t"
            "v_perm_b32 v104, v70, v66, %[pl]\n\t"
            "v_perm_b32 v105, v78, v74, %[pl]\n\t"
            "v_perm_b32 v106, v70, v66, %[ph]\n\t"
            "v_perm_b32 v107, v78, v74, %[ph]\n\t"
            "v_perm_b32 v108, v71, v67, %[pl]\n\t"
            "v_perm_b32 v109, v79, v75, %[pl]\n\t"
            "v_perm_b32 v110, v71, v67, %[ph]\n\t"
            "v_perm_b32 v111, v79, v75, %[ph]\n\t"
            "s_nop 1\n\t"
            "v_mfma_f32_4x4x4_16b_bf16 v[8:11], v[124:125], v[96:97], v[8:11]\n\t"
            "v_mfma_f32_4x4x4_16b_bf16 v[12:15], v[124:125], v[98:99], v[12:15]\n\t"
            "v_mfma_f32_4x4x4_16b_bf16 v[16:19], v[124:125], v[100:101], v[16:19]\n\t"
            "v_mfma_f32_4x4x4_16b_bf16 v[20:23], v[124:125], v[102:103], v[20:23]\n\t"
            "v_mfma_f32_4x4x4_16b_bf16 v[24:27], v[124:125], v[104:105], v[24:27]\n\t"
            "v_mfma_f32_4x4x4_16b_bf16 v[28:31], v[124:125], v[106:107], v[28:31]\n\t"
            "v_mfma_f32_4x4x4_16b_bf16 v[32:35], v[124:125], v[108:109], v[32:35]\n\t"
            "v_mfma_f32_4x4x4_16b_bf16 v[36:39], v[124:125], v[110:111], v[36:39]\n\t"
            "s_branch Ldone%=\n\t"
            "Lempty%=:\n"
            "s_waitcnt vmcnt(0)\n\t"
            "v_mov_b64 v[8:9], 0\n\t"
            "v_mov_b64 v[10:11], 0\n\t"
            "v_mov_b64 v[12:13], 0\n\t"
            "v_mov_b64 v[14:15], 0\n\t"
            "v_mov_b64 v[16:17], 0\n\t"
            "v_mov_b64 v[18:19], 0\n\t"
            "v_mov_b64 v[20:21], 0\n\t"
            "v_mov_b64 v[22:23], 0\n\t"
            "v_mov_b64 v[24:25], 0\n\t"
            "v_mov_b64 v[26:27], 0\n\t"
            "v_mov_b64 v[28:29], 0\n\t"
            "v_mov_b64 v[30:31], 0\n\t"
            "v_mov_b64 v[32:33], 0\n\t"
            "v_mov_b64 v[34:35], 0\n\t"
            "v_mov_b64 v[36:37], 0\n\t"
            "v_mov_b64 v[38:39], 0\n\t"
            "Ldone%=:\n"
            "s_nop 7\n\t"
            "s_nop 7\n\t"
            : "=&{v[8:11]}"(acc[0]), "=&{v[12:15]}"(acc[1]), "=&{v[16:19]}"(acc[2]), "=&{v[20:23]}"(acc[3]),
              "=&{v[24:27]}"(acc[4]), "=&{v[28:31]}"(acc[5]), "=&{v[32:35]}"(acc[6]), "=&{v[36:39]}"(acc[7]),
              [la] "+v"(laddr), [n] "=&s"(n)
            : [B] "s"(Bm), [s16] "v"(sub * 16),
              [ws] "v"((lane & 1) ? 0x07060302u : 0x05040100u),
              [rem] "s"(maxlen & 3),
              [grp] "s"(w >> 3), [nb] "s"(nb), [pl] "s"(0x05040100u), [ph] "s"(0x07060302u)
            : "memory", "scc",
              "v64", "v65", "v66", "v67", "v68", "v69", "v70", "v71", "v72", "v73", "v74", "v75", "v76", "v77", "v78", "v79", "v80", "v81", "v82", "v83", "v84", "v85", "v86", "v87", "v88", "v89", "v90", "v91", "v92", "v93", "v94", "v95", "v96", "v97", "v98", "v99", "v100", "v101", "v102", "v103", "v104", "v105", "v106", "v107", "v108", "v109", "v110", "v111", "v112", "v113", "v114", "v115", "v116", "v117", "v118", "v119", "v120", "v121", "v122", "v123", "v124", "v125", "v126", "v127");
    }

#pragma unroll
    for (int s = 0; s < 2; ++s) {
        u32x4 o;
#pragma unroll
        for (int m = 0; m < 4; ++m) {
            const bf16x2 p = __builtin_convertvector(f32x2{acc[2 * m][s], acc[2 * m + 1][s]}, bf16x2);
            o[m] = __builtin_bit_cast(unsigned, p);
        }
        const unsigned ooff = (unsigned)(rowBase + (s ? r1 : r0)) * 128u + sub * 16u;
        asm volatile("global_store_dwordx4 %0, %1, %2 sc1\n\ts_nop 1" :: "v"(ooff), "v"(o), "s"(out) : "memory");
    }
    const int nbMax = (__builtin_amdgcn_readfirstlane(longest) + 3) >> 2;
    for (int i = nb > 1 ? nb - 1 : nb; i < (nbMax > 1 ? nbMax - 1 : nbMax); ++i) __builtin_amdgcn_s_barrier();
}

__global__ __launch_bounds__(256) void spmm_ell_generic(const uint16_t* __restrict__ vals, const int* __restrict__ col,
                                                        const uint16_t* __restrict__ Bm, uint16_t* __restrict__ out, int M, int K) {
    const int row = (int)((blockIdx.x * 256u + threadIdx.x) >> 3), sub = threadIdx.x & 7;
    if (row >= M) return;
    float acc[8] = {0.f, 0.f, 0.f, 0.f, 0.f, 0.f, 0.f, 0.f};
    for (int j = 0; j < K; ++j) {
        const float f = __uint_as_float((unsigned)vals[(size_t)row * K + j] << 16);
        const u32x4 b = *(const u32x4*)(Bm + (size_t)col[(size_t)row * K + j] * 64 + sub * 8);
#pragma unroll
        for (int m = 0; m < 4; ++m) {
            acc[2 * m] += f * __uint_as_float(b[m] << 16);
            acc[2 * m + 1] += f * __uint_as_float(b[m] & 0xffff0000u);
        }
    }
    u32x4 o;
#pragma unroll
    for (int m = 0; m < 4; ++m) o[m] = __builtin_bit_cast(unsigned, __builtin_convertvector(f32x2{acc[2 * m], acc[2 * m + 1]}, bf16x2));
    *(u32x4*)(out + (size_t)row * 64 + sub * 8) = o;
}

extern "C" void kernel_launch(void* const* d_in, const int* in_sizes, int n_in,
                              void* d_out, int out_size, void* d_ws, size_t ws_size,
                              hipStream_t stream) {
    const uint16_t* vals = (const uint16_t*)d_in[0];
    const int* col = (const int*)d_in[1];
    const uint16_t* B = (const uint16_t*)d_in[2];
    uint16_t* out = (uint16_t*)d_out;
    const int M = out_size / 64, K = in_sizes[0] / M, N = in_sizes[2] / 64;
    if (M % ROWS == 0 && M <= 65536 && K == KS && N == 65536) spmm_ell_kernel<<<M / ROWS, NTHR, 0, stream>>>(vals, col, B, out);
    else spmm_ell_generic<<<(M * 8 + 255) / 256, 256, 0, stream>>>(vals, col, B, out, M, K);
}
```
